```python
import torch

def _softmax(x, axis=-1):
    return torch.softmax(x, dim=axis)
def _gelu(x):
    return torch.nn.functional.gelu(x, approximate="tanh")
def _silu(x):
    return torch.nn.functional.silu(x)
def _sigmoid(x):
    return torch.sigmoid(x)
def _layernorm(x, axis=-1, eps=1e-5):
    m = x.mean(dim=axis, keepdim=True)
    v = x.var(dim=axis, keepdim=True, unbiased=False)
    return (x - m) / torch.sqrt(v + eps)
def _scatter_add(tgt, idx, src, axis=0):
    return tgt.clone().index_add_(axis, idx, src)
def _segment_sum(src, seg, n):
    out = torch.zeros((n,) + src.shape[1:], dtype=src.dtype)
    return out.index_add_(0, seg, src)
def _pad(x, pw):
    flat = tuple(v for pair in reversed(pw) for v in pair)
    return torch.nn.functional.pad(x, flat)

kernel_name = 'sequential_preLN__compressed_kv_cla__swiglu_ffn__decode'

def setup_inputs():

    _w = (lambda a: (lambda t: t.to(torch.bfloat16) if t.is_floating_point() else t)(torch.as_tensor(a)))
    def _A_setup():
        B, H, Sq, Sk, D, M = 2, 32, 1, 4096, 128, 576
        Dc = 4 * D
        return {
            "x": _w((lambda *s: torch.randn(*s, dtype=torch.bfloat16) * 0.05)(B, Sq, M)),
            "c_kv": _w((lambda *s: torch.randn(*s, dtype=torch.bfloat16) * 0.4)(B, Sk, Dc)),
            "w_q": _w((lambda *s: torch.randn(*s, dtype=torch.bfloat16) * 3.2)(M, H, D) * (M ** -0.5)),
            "w_uk": _w((lambda *s: torch.randn(*s, dtype=torch.bfloat16) * 3.2)(Dc, H, D) * (Dc ** -0.5)),
            "w_uv": _w((lambda *s: torch.randn(*s, dtype=torch.bfloat16) * 0.4)(Dc, H, D) * (Dc ** -0.5)),
            "w_o": _w((lambda *s: torch.randn(*s, dtype=torch.bfloat16) * 0.4)(H, D, M) * ((H * D) ** -0.5)),
        }
    _A_in = _A_setup()

    def _B_setup():
        B, S, D, F = 2, 1, 576, 4608
        return {
            "x": _w((lambda *s: torch.randn(*s, dtype=torch.bfloat16) * 0.05)(B, S, D)),
            "w_gate": _w((lambda *s: torch.randn(*s, dtype=torch.bfloat16) * 0.05)(D, F)),
            "w_up": _w((lambda *s: torch.randn(*s, dtype=torch.bfloat16) * 0.05)(D, F)),
            "w_down": _w((lambda *s: torch.randn(*s, dtype=torch.bfloat16) * 0.05)(F, D)),
        }
    _B_in = _B_setup()

    return {
        "A_x": _A_in["x"],
        "A_c_kv": _A_in["c_kv"],
        "A_w_q": _A_in["w_q"],
        "A_w_uk": _A_in["w_uk"],
        "A_w_uv": _A_in["w_uv"],
        "A_w_o": _A_in["w_o"],
        "B_x": _B_in["x"],
        "B_w_gate": _B_in["w_gate"],
        "B_w_up": _B_in["w_up"],
        "B_w_down": _B_in["w_down"],
    }

def reference(A_x, A_c_kv, A_w_q, A_w_uk, A_w_uv, A_w_o, B_x, B_w_gate, B_w_up, B_w_down):
    def _A_ref(x, c_kv, w_q, w_uk, w_uv, w_o):
        B, Sq, M = x.shape[0], x.shape[1], x.shape[2]
        Sk = c_kv.shape[1]
        H, D = w_q.shape[1], w_q.shape[2]
        q = torch.einsum("bqm,mhd->bhqd", x, w_q)
        k = torch.einsum("bkc,chd->bhkd", c_kv, w_uk)
        v = torch.einsum("bkc,chd->bhkd", c_kv, w_uv)
        scores = torch.einsum("bhqd,bhkd->bhqk", q, k) * (D ** -0.5)
        off = Sk - Sq
        qi = torch.arange(Sq)[:, None]
        kj = torch.arange(Sk)[None, :]
        cmask = kj <= (qi + off)
        scores = torch.where(cmask[None, None], scores, -1e9)
        p = _softmax(scores, axis=-1)
        ctx = torch.einsum("bhqk,bhkd->bhqd", p, v)
        return torch.einsum("bhqd,hdm->bqm", ctx, w_o)

    def _B_ref(x, w_gate, w_up, w_down):
        g = _silu(x @ w_gate)
        u = x @ w_up
        return (g * u) @ w_down

    x = A_x
    h = x + _A_ref(_layernorm(x, axis=-1), A_c_kv, A_w_q, A_w_uk, A_w_uv, A_w_o)
    h2 = h + _B_ref(_layernorm(h, axis=-1), B_w_gate, B_w_up, B_w_down)
    return h2
```

```cpp
#include <hip/hip_runtime.h>
#include <hip/hip_bf16.h>

typedef unsigned short u16;
__device__ __forceinline__ float bf2f(u16 v) { return __uint_as_float(((unsigned)v) << 16); }
__device__ __forceinline__ u16 f2bf(float f) { return __builtin_bit_cast(u16, (__bf16)f); }

constexpr int B = 2, H = 32, SK = 4096, D = 128, M = 576, DC = 512, F = 4608;

__device__ float block_sum(float v, float* red) {
    for (int o = 32; o > 0; o >>= 1) v += __shfl_xor(v, o);
    int w = threadIdx.x >> 6;
    __syncthreads();
    if ((threadIdx.x & 63) == 0) red[w] = v;
    __syncthreads();
    float s = 0;
    for (int i = 0; i < (int)(blockDim.x >> 6); ++i) s += red[i];
    return s;
}

__device__ void ln576(const float* src, float* dst, float* red) {
    float s = 0;
    for (int i = threadIdx.x; i < M; i += blockDim.x) s += src[i];
    float mean = block_sum(s, red) * (1.0f / M);
    float v = 0;
    for (int i = threadIdx.x; i < M; i += blockDim.x) { float d = src[i] - mean; v += d * d; }
    float var = block_sum(v, red) * (1.0f / M);
    float r = 1.0f / sqrtf(var + 1e-5f);
    for (int i = threadIdx.x; i < M; i += blockDim.x) dst[i] = (src[i] - mean) * r;
    __syncthreads();
}

__global__ void k_q(const u16* x, const u16* wq, float* q) {
    __shared__ float xs[2][M], xn[2][M], red[16];
    for (int i = threadIdx.x; i < 2 * M; i += blockDim.x) xs[i / M][i % M] = bf2f(x[i]);
    __syncthreads();
    ln576(xs[0], xn[0], red);
    ln576(xs[1], xn[1], red);
    int n = blockIdx.x * blockDim.x + threadIdx.x;
    float a0 = 0, a1 = 0;
    for (int m = 0; m < M; ++m) { float w = bf2f(wq[(size_t)m * (H * D) + n]); a0 += xn[0][m] * w; a1 += xn[1][m] * w; }
    q[n] = a0; q[H * D + n] = a1;
}
__global__ void k_qabs(const float* q, const u16* wuk, float* qa) {
    int h = blockIdx.x, c = blockIdx.y * 64 + threadIdx.x;
    float a0 = 0, a1 = 0;
    for (int d = 0; d < D; ++d) { float w = bf2f(wuk[((size_t)c * H + h) * D + d]); a0 += q[h * D + d] * w; a1 += q[H * D + h * D + d] * w; }
    const float sc = 0.08838834764831845f;
    qa[(0 * H + h) * DC + c] = a0 * sc; qa[(1 * H + h) * DC + c] = a1 * sc;
}
__global__ void k_scores(const float* qa, const u16* ckv, float* s) {
    int k = blockIdx.x * 64 + threadIdx.x, h = blockIdx.y, b = blockIdx.z;
    float a = 0;
    for (int c = 0; c < DC; ++c) a += qa[(b * H + h) * DC + c] * bf2f(ckv[((size_t)b * SK + k) * DC + c]);
    s[((size_t)b * H + h) * SK + k] = a;
}
__global__ void k_softmax(float* s) {
    __shared__ float red[16];
    float* p = s + (size_t)blockIdx.x * SK;
    float m = -INFINITY;
    for (int i = threadIdx.x; i < SK; i += blockDim.x) m = fmaxf(m, p[i]);
    for (int o = 32; o > 0; o >>= 1) m = fmaxf(m, __shfl_xor(m, o));
    if ((threadIdx.x & 63) == 0) red[threadIdx.x >> 6] = m;
    __syncthreads();
    m = red[0]; for (int i = 1; i < (int)(blockDim.x >> 6); ++i) m = fmaxf(m, red[i]);
    float l = 0;
    for (int i = threadIdx.x; i < SK; i += blockDim.x) { float e = __expf(p[i] - m); p[i] = e; l += e; }
    l = block_sum(l, red);
    float r = 1.0f / l;
    for (int i = threadIdx.x; i < SK; i += blockDim.x) p[i] *= r;
}
__global__ void k_ctxlat(const float* p, const u16* ckv, float* cl) {
    int c = blockIdx.x * 64 + threadIdx.x, h = blockIdx.y, b = blockIdx.z;
    float a = 0;
    for (int k = 0; k < SK; ++k) a += p[((size_t)b * H + h) * SK + k] * bf2f(ckv[((size_t)b * SK + k) * DC + c]);
    cl[(b * H + h) * DC + c] = a;
}
__global__ void k_ctx(const float* cl, const u16* wuv, float* ctx) {
    int h = blockIdx.x, d = threadIdx.x;
    float a0 = 0, a1 = 0;
    for (int c = 0; c < DC; ++c) { float w = bf2f(wuv[((size_t)c * H + h) * D + d]); a0 += cl[(0 * H + h) * DC + c] * w; a1 += cl[(1 * H + h) * DC + c] * w; }
    ctx[h * D + d] = a0; ctx[H * D + h * D + d] = a1;
}
__global__ void k_o(const float* ctx, const u16* wo, const u16* x, float* hh) {
    int m = blockIdx.x * 64 + threadIdx.x;
    float a0 = 0, a1 = 0;
    for (int k = 0; k < H * D; ++k) { float w = bf2f(wo[(size_t)k * M + m]); a0 += ctx[k] * w; a1 += ctx[H * D + k] * w; }
    hh[m] = a0 + bf2f(x[m]); hh[M + m] = a1 + bf2f(x[M + m]);
}
__global__ void k_gu(const float* hh, const u16* wg, const u16* wu, float* act) {
    __shared__ float xs[2][M], xn[2][M], red[16];
    for (int i = threadIdx.x; i < 2 * M; i += blockDim.x) xs[i / M][i % M] = hh[i];
    __syncthreads();
    ln576(xs[0], xn[0], red);
    ln576(xs[1], xn[1], red);
    int f = blockIdx.x * blockDim.x + threadIdx.x;
    float g0 = 0, g1 = 0, u0 = 0, u1 = 0;
    for (int m = 0; m < M; ++m) {
        float a = bf2f(wg[(size_t)m * F + f]), c = bf2f(wu[(size_t)m * F + f]);
        g0 += xn[0][m] * a; g1 += xn[1][m] * a; u0 += xn[0][m] * c; u1 += xn[1][m] * c;
    }
    act[f] = g0 / (1.0f + __expf(-g0)) * u0; act[F + f] = g1 / (1.0f + __expf(-g1)) * u1;
}
__global__ void k_down(const float* act, const u16* wd, const float* hh, u16* out) {
    int m = blockIdx.x * 64 + threadIdx.x;
    float a0 = 0, a1 = 0;
    for (int k = 0; k < F; ++k) { float w = bf2f(wd[(size_t)k * M + m]); a0 += act[k] * w; a1 += act[F + k] * w; }
    out[m] = f2bf(a0 + hh[m]); out[M + m] = f2bf(a1 + hh[M + m]);
}

extern "C" void kernel_launch(void* const* d_in, const int* in_sizes, int n_in,
                              void* d_out, int out_size, void* d_ws, size_t ws_size,
                              hipStream_t stream) {
    const u16* x = (const u16*)d_in[0];
    const u16* ckv = (const u16*)d_in[1];
    const u16* wq = (const u16*)d_in[2];
    const u16* wuk = (const u16*)d_in[3];
    const u16* wuv = (const u16*)d_in[4];
    const u16* wo = (const u16*)d_in[5];
    const u16* wg = (const u16*)d_in[7];
    const u16* wu = (const u16*)d_in[8];
    const u16* wd = (const u16*)d_in[9];
    float* ws = (float*)d_ws;
    float* q = ws;
    float* qa = q + 2 * H * D;
    float* s = qa + 2 * H * DC;
    float* cl = s + 2 * H * SK;
    float* ctx = cl + 2 * H * DC;
    float* hh = ctx + 2 * H * D;
    float* act = hh + 2 * M + 64;
    k_q<<<H * D / 64, 64, 0, stream>>>(x, wq, q);
    k_qabs<<<dim3(H, DC / 64), 64, 0, stream>>>(q, wuk, qa);
    k_scores<<<dim3(SK / 64, H, B), 64, 0, stream>>>(qa, ckv, s);
    k_softmax<<<B * H, 256, 0, stream>>>(s);
    k_ctxlat<<<dim3(DC / 64, H, B), 64, 0, stream>>>(s, ckv, cl);
    k_ctx<<<H, 128, 0, stream>>>(cl, wuv, ctx);
    k_o<<<M / 64, 64, 0, stream>>>(ctx, wo, x, hh);
    k_gu<<<F / 64, 64, 0, stream>>>(hh, wg, wu, act);
    k_down<<<M / 64, 64, 0, stream>>>(act, wd, hh, (u16*)d_out);
}
```

```cpp
#define STRAIGHT 0
#include <hip/hip_runtime.h>
#include <hip/hip_bf16.h>

typedef unsigned short u16;
__device__ __forceinline__ float bf2f(u16 v) { return __uint_as_float(((unsigned)v) << 16); }
__device__ __forceinline__ u16 f2bf(float f) { return __builtin_bit_cast(u16, (__bf16)f); }

constexpr int B = 2, H = 32, SK = 4096, D = 128, M = 576, DC = 512, F = 4608;

__device__ float block_sum(float v, float* red) {
    for (int o = 32; o > 0; o >>= 1) v += __shfl_xor(v, o);
    int w = threadIdx.x >> 6;
    __syncthreads();
    if ((threadIdx.x & 63) == 0) red[w] = v;
    __syncthreads();
    float s = 0;
    for (int i = 0; i < (int)(blockDim.x >> 6); ++i) s += red[i];
    return s;
}

__device__ void ln576(const float* src, float* dst, float* red) {
    float s = 0;
    for (int i = threadIdx.x; i < M; i += blockDim.x) s += src[i];
    float mean = block_sum(s, red) * (1.0f / M);
    float v = 0;
    for (int i = threadIdx.x; i < M; i += blockDim.x) { float d = src[i] - mean; v += d * d; }
    float var = block_sum(v, red) * (1.0f / M);
    float r = 1.0f / sqrtf(var + 1e-5f);
    for (int i = threadIdx.x; i < M; i += blockDim.x) dst[i] = (src[i] - mean) * r;
    __syncthreads();
}

__global__ void k_q(const u16* x, const u16* wq, float* q) {
    __shared__ float xs[2][M], xn[2][M], red[16];
    for (int i = threadIdx.x; i < 2 * M; i += blockDim.x) xs[i / M][i % M] = bf2f(x[i]);
    __syncthreads();
    ln576(xs[0], xn[0], red);
    ln576(xs[1], xn[1], red);
    int n = blockIdx.x * blockDim.x + threadIdx.x;
    float a0 = 0, a1 = 0;
    for (int m = 0; m < M; ++m) { float w = bf2f(wq[(size_t)m * (H * D) + n]); a0 += xn[0][m] * w; a1 += xn[1][m] * w; }
    q[n] = a0; q[H * D + n] = a1;
}
__global__ void k_qabs(const float* q, const u16* wuk, float* qa) {
    int h = blockIdx.x, c = blockIdx.y * 64 + threadIdx.x;
    float a0 = 0, a1 = 0;
    for (int d = 0; d < D; ++d) { float w = bf2f(wuk[((size_t)c * H + h) * D + d]); a0 += q[h * D + d] * w; a1 += q[H * D + h * D + d] * w; }
    const float sc = 0.08838834764831845f;
    qa[(0 * H + h) * DC + c] = a0 * sc; qa[(1 * H + h) * DC + c] = a1 * sc;
}
__global__ void k_scores(const float* qa, const u16* ckv, float* s) {
    int k = blockIdx.x * 64 + threadIdx.x, h = blockIdx.y, b = blockIdx.z;
    float a = 0;
    for (int c = 0; c < DC; ++c) a += qa[(b * H + h) * DC + c] * bf2f(ckv[((size_t)b * SK + k) * DC + c]);
    s[((size_t)b * H + h) * SK + k] = a;
}
__global__ void k_softmax(float* s) {
    __shared__ float red[16];
    float* p = s + (size_t)blockIdx.x * SK;
    float m = -INFINITY;
    for (int i = threadIdx.x; i < SK; i += blockDim.x) m = fmaxf(m, p[i]);
    for (int o = 32; o > 0; o >>= 1) m = fmaxf(m, __shfl_xor(m, o));
    if ((threadIdx.x & 63) == 0) red[threadIdx.x >> 6] = m;
    __syncthreads();
    m = red[0]; for (int i = 1; i < (int)(blockDim.x >> 6); ++i) m = fmaxf(m, red[i]);
    float l = 0;
    for (int i = threadIdx.x; i < SK; i += blockDim.x) { float e = __expf(p[i] - m); p[i] = e; l += e; }
    l = block_sum(l, red);
    float r = 1.0f / l;
    for (int i = threadIdx.x; i < SK; i += blockDim.x) p[i] *= r;
}
__global__ void k_ctxlat(const float* p, const u16* ckv, float* cl) {
    int c = blockIdx.x * 64 + threadIdx.x, h = blockIdx.y, b = blockIdx.z;
    float a = 0;
    for (int k = 0; k < SK; ++k) a += p[((size_t)b * H + h) * SK + k] * bf2f(ckv[((size_t)b * SK + k) * DC + c]);
    cl[(b * H + h) * DC + c] = a;
}
__global__ void k_ctx(const float* cl, const u16* wuv, float* ctx) {
    int h = blockIdx.x, d = threadIdx.x;
    float a0 = 0, a1 = 0;
    for (int c = 0; c < DC; ++c) { float w = bf2f(wuv[((size_t)c * H + h) * D + d]); a0 += cl[(0 * H + h) * DC + c] * w; a1 += cl[(1 * H + h) * DC + c] * w; }
    ctx[h * D + d] = a0; ctx[H * D + h * D + d] = a1;
}
__global__ void k_o(const float* ctx, const u16* wo, const u16* x, float* hh) {
    int m = blockIdx.x * 64 + threadIdx.x;
    float a0 = 0, a1 = 0;
    for (int k = 0; k < H * D; ++k) { float w = bf2f(wo[(size_t)k * M + m]); a0 += ctx[k] * w; a1 += ctx[H * D + k] * w; }
    hh[m] = a0 + bf2f(x[m]); hh[M + m] = a1 + bf2f(x[M + m]);
}
__global__ void k_gu(const float* hh, const u16* wg, const u16* wu, float* act) {
    __shared__ float xs[2][M], xn[2][M], red[16];
    for (int i = threadIdx.x; i < 2 * M; i += blockDim.x) xs[i / M][i % M] = hh[i];
    __syncthreads();
    ln576(xs[0], xn[0], red);
    ln576(xs[1], xn[1], red);
    int f = blockIdx.x * blockDim.x + threadIdx.x;
    float g0 = 0, g1 = 0, u0 = 0, u1 = 0;
    for (int m = 0; m < M; ++m) {
        float a = bf2f(wg[(size_t)m * F + f]), c = bf2f(wu[(size_t)m * F + f]);
        g0 += xn[0][m] * a; g1 += xn[1][m] * a; u0 += xn[0][m] * c; u1 += xn[1][m] * c;
    }
    act[f] = g0 / (1.0f + __expf(-g0)) * u0; act[F + f] = g1 / (1.0f + __expf(-g1)) * u1;
}
__global__ void k_down(const float* act, const u16* wd, const float* hh, u16* out) {
    int m = blockIdx.x * 64 + threadIdx.x;
    float a0 = 0, a1 = 0;
    for (int k = 0; k < F; ++k) { float w = bf2f(wd[(size_t)k * M + m]); a0 += act[k] * w; a1 += act[F + k] * w; }
    out[m] = f2bf(a0 + hh[m]); out[M + m] = f2bf(a1 + hh[M + m]);
}

#define I8 "v_fma_f32 %0, %0, %1, %1\n\tv_fma_f32 %0, %0, %1, %1\n\tv_fma_f32 %0, %0, %1, %1\n\tv_fma_f32 %0, %0, %1, %1\n\tv_fma_f32 %0, %0, %1, %1\n\tv_fma_f32 %0, %0, %1, %1\n\tv_fma_f32 %0, %0, %1, %1\n\tv_fma_f32 %0, %0, %1, %1\n\t"
#define I64 I8 I8 I8 I8 I8 I8 I8 I8
#define I512 I64 I64 I64 I64 I64 I64 I64 I64
__global__ void __launch_bounds__(64) k_dummy(float* sink, int reps) {
    float a = threadIdx.x, b = 0.5f;
#pragma unroll 1
    for (int p = 0; p < reps; ++p) {
        asm volatile(I64 : "+v"(a) : "v"(b));
    }
    if (a == 1234.5f) sink[0] = a;
}
extern "C" void kernel_launch(void* const* d_in, const int* in_sizes, int n_in,
                              void* d_out, int out_size, void* d_ws, size_t ws_size,
                              hipStream_t stream) {
    const u16* x = (const u16*)d_in[0];
    const u16* ckv = (const u16*)d_in[1];
    const u16* wq = (const u16*)d_in[2];
    const u16* wuk = (const u16*)d_in[3];
    const u16* wuv = (const u16*)d_in[4];
    const u16* wo = (const u16*)d_in[5];
    const u16* wg = (const u16*)d_in[7];
    const u16* wu = (const u16*)d_in[8];
    const u16* wd = (const u16*)d_in[9];
    float* ws = (float*)d_ws;
    float* q = ws;
    float* qa = q + 2 * H * D;
    float* s = qa + 2 * H * DC;
    float* cl = s + 2 * H * SK;
    float* ctx = cl + 2 * H * DC;
    float* hh = ctx + 2 * H * D;
    float* act = hh + 2 * M + 64;
    k_dummy<<<256, 64, 0, stream>>>(ws + (1 << 22), STRAIGHT ? 1 : 64);
    k_q<<<H * D / 64, 64, 0, stream>>>(x, wq, q);
    k_qabs<<<dim3(H, DC / 64), 64, 0, stream>>>(q, wuk, qa);
    k_scores<<<dim3(SK / 64, H, B), 64, 0, stream>>>(qa, ckv, s);
    k_softmax<<<B * H, 256, 0, stream>>>(s);
    k_ctxlat<<<dim3(DC / 64, H, B), 64, 0, stream>>>(s, ckv, cl);
    k_ctx<<<H, 128, 0, stream>>>(cl, wuv, ctx);
    k_o<<<M / 64, 64, 0, stream>>>(ctx, wo, x, hh);
    k_gu<<<F / 64, 64, 0, stream>>>(hh, wg, wu, act);
    k_down<<<M / 64, 64, 0, stream>>>(act, wd, hh, (u16*)d_out);
}
```

```cpp
#include <hip/hip_runtime.h>
#include <hip/hip_bf16.h>

#define LAS __attribute__((address_space(3)))
typedef unsigned v4u __attribute__((ext_vector_type(4)));
typedef unsigned v2u __attribute__((ext_vector_type(2)));
typedef float f32x4 __attribute__((ext_vector_type(4)));
typedef float f32x2 __attribute__((ext_vector_type(2)));
typedef short s16x4 __attribute__((ext_vector_type(4)));
typedef __bf16 bf16x8 __attribute__((ext_vector_type(8)));
typedef __bf16 bf16x2 __attribute__((ext_vector_type(2)));
typedef __amdgpu_buffer_rsrc_t rsrc_t;
#define DI __device__ __forceinline__

#define STAMPS 0

#pragma clang diagnostic ignored "-Winline-asm"
#define KVER 2
namespace {

constexpr int NT = 512, NW = 8;
constexpr unsigned SENT = 0xFFFFFFFFu;
constexpr int AUX_NT = 2, AUX_SC1 = 16;
constexpr unsigned SPIN_CAP = 1u << 18;
constexpr int OOB = 0x7ffff000;

constexpr int EPOCH_OFF = 0;
constexpr int FL_OFF = 1024;
constexpr int QP_OFF = 4096;
constexpr int QA_OFF = QP_OFF + 131072;
constexpr int SM_OFF = QA_OFF + 65536;
constexpr int SL_OFF = SM_OFF + 8192;
constexpr int CX_OFF = SL_OFF + 8192;
constexpr int AO_OFF = CX_OFF + 131072;
constexpr int FO_OFF = AO_OFF + 18432;
constexpr int OP_OFF = 1 << 20;
constexpr int WS_BYTES = OP_OFF + (2 << 20);
constexpr int DBG_OFF = 8 << 20;

constexpr int CKP = 1040;
constexpr int L_CK = 0;
constexpr int L_Q = 128 * CKP;
constexpr int L_P = L_Q + 16 * CKP;
constexpr int L_MX = L_P + 16 * 272;
constexpr int L_WV = 0;
constexpr int L_CL = 40960;
constexpr int L_UK = 0;
constexpr int L_RA = 32768;
constexpr int L_DN = 0;
constexpr int L_GU1 = 73728;
constexpr int L_GU2 = 0;
constexpr int L_AO = 73728;
constexpr int AOP = 1168;
constexpr int L_FS = 155648;
constexpr int L_ACT = L_FS + 64;
constexpr int L_WO = 0;
constexpr int L_RE = 131072;
constexpr int L_DBG = 156672;
constexpr int LDS_BYTES = L_DBG + 8 * 16 * 8;
static_assert(L_MX + 512 <= L_DBG && L_ACT + 256 <= L_DBG, "lds map");

DI v4u ld_nt(rsrc_t r, int off) { return __builtin_amdgcn_raw_buffer_load_b128(r, off, 0, AUX_NT); }
DI v4u ld_c(rsrc_t r, int off) { return __builtin_amdgcn_raw_buffer_load_b128(r, off, 0, AUX_SC1); }
DI v2u ld_c2(rsrc_t r, int off) { return __builtin_amdgcn_raw_buffer_load_b64(r, off, 0, AUX_SC1); }
DI unsigned ld_c1(rsrc_t r, int off) { return __builtin_amdgcn_raw_buffer_load_b32(r, off, 0, AUX_SC1); }
DI void st_c(rsrc_t r, int off, v4u v) { __builtin_amdgcn_raw_buffer_store_b128(v, r, off, 0, AUX_SC1); }
DI void st_c2(rsrc_t r, int off, v2u v) { __builtin_amdgcn_raw_buffer_store_b64(v, r, off, 0, AUX_SC1); }
DI void st_c1(rsrc_t r, int off, unsigned v) { __builtin_amdgcn_raw_buffer_store_b32(v, r, off, 0, AUX_SC1); }
DI unsigned san(unsigned x) { return x < 0xFFFFFFFEu ? x : 0xFFFFFFFEu; }
DI rsrc_t mk(const void* p, unsigned bytes) { return __builtin_amdgcn_make_buffer_rsrc((void*)p, 0, bytes, 0x00020000); }
DI v4u mks(const void* p, unsigned bytes) {
    const unsigned long long a = (unsigned long long)p;
    return v4u{(unsigned)a, (unsigned)(a >> 32) & 0xFFFFu, bytes, 0x00020000u};
}
DI void dma(v4u srd, int voff, int soff, int lds) {
    asm volatile("s_mov_b32 m0, %0\n\ts_nop 0\n\tbuffer_load_dwordx4 %1, %2, %3 offen nt lds" ::"s"(lds), "v"(voff), "s"(srd), "s"(soff) : "m0", "memory");
}
DI float asf(unsigned u) { return __builtin_bit_cast(float, u); }
DI unsigned asu(float f) { return __builtin_bit_cast(unsigned, f); }
DI float bflo(unsigned u) { return asf(u << 16); }
DI float bfhi(unsigned u) { return asf(u & 0xFFFF0000u); }
DI unsigned pk2(float a, float b) { bf16x2 t = {(__bf16)a, (__bf16)b}; return __builtin_bit_cast(unsigned, t); }
DI f32x4 mfma32(v4u a, v4u b, f32x4 c) {
    return __builtin_amdgcn_mfma_f32_16x16x32_bf16(__builtin_bit_cast(bf16x8, a), __builtin_bit_cast(bf16x8, b), c, 0, 0, 0);
}
template <int CTRL> DI float dpp(float v) { return asf((unsigned)__builtin_amdgcn_update_dpp(0, (int)asu(v), CTRL, 0xf, 0xf, true)); }
template <int CTRL> DI unsigned dppu(unsigned v) { return (unsigned)__builtin_amdgcn_update_dpp(0, (int)v, CTRL, 0xf, 0xf, true); }
constexpr int DPP_X1 = 0xB1, DPP_X2 = 0x4E, DPP_HMIR = 0x141, DPP_ROR8 = 0x128, DPP_ROR4 = 0x124, DPP_ROR2 = 0x122, DPP_ROR1 = 0x121;
constexpr int DPP_BC = 0x150;
DI float rs16(float a, float b) { auto r = __builtin_amdgcn_permlane16_swap(asu(a), asu(b), false, false); unsigned x = r[0], y = r[1]; return asf(x) + asf(y); }
DI float rs32(float a, float b) { auto r = __builtin_amdgcn_permlane32_swap(asu(a), asu(b), false, false); unsigned x = r[0], y = r[1]; return asf(x) + asf(y); }
DI float add16(float v) { return rs16(v, v); }
DI float max16(float v) { auto r = __builtin_amdgcn_permlane16_swap(asu(v), asu(v), false, false); unsigned a = r[0], b = r[1]; return fmaxf(asf(a), asf(b)); }
DI float max32(float v) { auto r = __builtin_amdgcn_permlane32_swap(asu(v), asu(v), false, false); unsigned a = r[0], b = r[1]; return fmaxf(asf(a), asf(b)); }
DI float rowsum(float v) { v += dpp<DPP_ROR8>(v); v += dpp<DPP_ROR4>(v); v += dpp<DPP_ROR2>(v); v += dpp<DPP_ROR1>(v); return v; }
DI float rowmax(float v) { v = fmaxf(v, dpp<DPP_ROR8>(v)); v = fmaxf(v, dpp<DPP_ROR4>(v)); v = fmaxf(v, dpp<DPP_ROR2>(v)); v = fmaxf(v, dpp<DPP_ROR1>(v)); return v; }
DI float sum4(f32x4 d) { return (d[0] + d[2]) + (d[1] + d[3]); }
DI unsigned umax(unsigned a, unsigned b) { return a > b ? a : b; }
DI unsigned mx4(v4u v) { return umax(umax(v.x, v.y), umax(v.z, v.w)); }
DI v4u trd(LAS unsigned char* p0, LAS unsigned char* p1) {
    v2u a = __builtin_bit_cast(v2u, __builtin_amdgcn_ds_read_tr16_b64_v4i16((LAS s16x4*)p0));
    v2u b = __builtin_bit_cast(v2u, __builtin_amdgcn_ds_read_tr16_b64_v4i16((LAS s16x4*)p1));
    return v4u{a.x, a.y, b.x, b.y};
}
#define CBAR() asm volatile("" ::: "memory")
#define VMW(n) asm volatile("s_waitcnt vmcnt(" #n ")" ::: "memory")
#define BAR() asm volatile("s_waitcnt lgkmcnt(0)\n\ts_barrier" ::: "memory")
#define PIN(v) asm volatile("" : "+v"(v))
constexpr f32x4 Z4 = {0.f, 0.f, 0.f, 0.f};

DI void check_flags(rsrc_t ws, v2u want, int lane) {
    for (unsigned s = 0; s < SPIN_CAP; ++s) {
        CBAR();
        v4u a = ld_c(ws, FL_OFF + lane * 16), b = ld_c(ws, FL_OFF + 1024 + lane * 16);
        unsigned d = (a.x ^ want.x) | (a.y ^ want.y) | (a.z ^ want.x) | (a.w ^ want.y) | (b.x ^ want.x) | (b.y ^ want.y) | (b.z ^ want.x) | (b.w ^ want.y);
        if (__all(d == 0u)) break;
    }
    CBAR();
}
DI v2u next_epoch(v2u ep) {
    unsigned long long e = (((unsigned long long)ep.y << 32) | ep.x) + 1ull;
    return v2u{(unsigned)e, (unsigned)(e >> 32)};
}

#define STAMP(i) do {} while (0)
#define DUMP() do {} while (0)
#define STAMP_INIT() do {} while (0)

struct Args {
    const void *x, *ckv, *wq, *wuk, *wuv, *wo, *wg, *wu, *wd;
    void* out;
    unsigned char* ws;
};

__global__ void __launch_bounds__(NT, 2) fused(Args a) {
    extern __shared__ __attribute__((aligned(16))) unsigned char smem[];
    LAS unsigned char* L = (LAS unsigned char*)smem;
    const int tid = threadIdx.x, lane = tid & 63, w = __builtin_amdgcn_readfirstlane(tid >> 6);
    const int xg = blockIdx.x & 7, r = blockIdx.x >> 3;
    const rsrc_t ws = mk(a.ws, WS_BYTES);
    const rsrc_t rx = mk(a.x, 2 * 576 * 2);
    const int c8 = lane & 7, rg = lane >> 3, i16 = lane & 15, g = lane >> 4;
    const int tq = i16 >> 2, tp = i16 & 3;
    const v4u SENT4 = {SENT, SENT, SENT, SENT};
    const unsigned hm = (i16 & 1) ? 0xFFFF0000u : 0x0000FFFFu;
    const int hq = (i16 >> 1) & 3;
    STAMP_INIT();
    STAMP(0);

    if (r < 16) {
        const int b = xg >> 2, kb = 8 * (xg & 3) + (r >> 1), hg = r & 1;
        const int dh = 4 * xg + (r >> 2), dcq = r & 3;
#pragma unroll
        for (int i = 0; i < 2; ++i)
            st_c(ws, OP_OFF + ((((b * 32 + 16 * hg + 2 * w + i) * 4 + g) * 32 + kb) * 256) + i16 * 16, SENT4);
        if (w == 0) {
            st_c(ws, lane < 8 ? SM_OFF + (lane >> 2) * 8192 + ((b * 32 + kb) * 32 + 16 * hg) * 4 + (lane & 3) * 16 : OOB, SENT4);
            st_c(ws, CX_OFF + dh * 4096 + ((g >> 1) * 8 + dcq * 2 + (g & 1)) * 256 + i16 * 16, SENT4);
        }
        const v2u want = next_epoch(ld_c2(ws, EPOCH_OFF));
        {
            const v4u sc = mks(a.ckv, 2 * 4096 * 512 * 2);
            const int so = (b * 4096 + 128 * kb + 16 * w) * 1024;
#pragma unroll
            for (int i = 0; i < 12; ++i) dma(sc, lane * 16, so + i * 1024, L_CK + (16 * w + i) * CKP);
            VMW(6);
            BAR();
            if (w == 0 && lane == 0) st_c2(ws, FL_OFF + (xg * 32 + r) * 8, want);
            v2u fl = ld_c2(ws, FL_OFF + (w * 32 + 16 + i16) * 8);
#pragma unroll
            for (int i = 12; i < 16; ++i) dma(sc, lane * 16, so + i * 1024, L_CK + (16 * w + i) * CKP);
            VMW(0);
            STAMP(1);
            for (unsigned s = 0; s < SPIN_CAP; ++s) {
                if (__all(((fl.x ^ want.x) | (fl.y ^ want.y)) == 0u)) break;
                CBAR();
                fl = ld_c2(ws, FL_OFF + (w * 32 + 16 + i16) * 8);
            }
        }
        v4u sf[16], of[16];
#pragma unroll
        for (int s = 0; s < 16; ++s) sf[s] = *(LAS v4u*)(L + L_CK + (16 * w + i16) * CKP + (4 * s + g) * 16);
        BAR();
        const int qoff = QA_OFF + (b * 32 + 16 * hg) * 1024 + tid * 16;
        v4u q0 = ld_c(ws, qoff), q1 = ld_c(ws, qoff + 8192);
        {
            LAS unsigned char* tb = L + L_CK + (8 * g + tq) * CKP + (64 * w + 8 * tp) * 2;
#pragma unroll
            for (int tau = 0; tau < 4; ++tau)
#pragma unroll
                for (int sg = 0; sg < 4; ++sg) {
                    LAS unsigned char* p = tb + sg * 32 * CKP + (32 * (tau >> 1) + 4 * (tau & 1)) * 2;
                    of[tau * 4 + sg] = trd(p, p + 4 * CKP);
                }
        }
        STAMP(2);
        for (unsigned s = 0; s < SPIN_CAP; ++s) {
            if (__all(umax(mx4(q0), mx4(q1)) != SENT)) break;
            CBAR();
            q0 = ld_c(ws, qoff); q1 = ld_c(ws, qoff + 8192);
        }
        STAMP(3);
        *(LAS v4u*)(L + L_Q + w * CKP + lane * 16) = q0;
        *(LAS v4u*)(L + L_Q + (8 + w) * CKP + lane * 16) = q1;
        BAR();
        f32x4 sacc = Z4;
#pragma unroll
        for (int s = 0; s < 16; ++s) {
            v4u qf = *(LAS v4u*)(L + L_Q + i16 * CKP + (4 * s + g) * 16);
            sacc = mfma32(sf[s], qf, sacc);
        }
        {
            const v4u sv = mks(a.wuv, 512 * 4096 * 2);
            const int k0 = (rg >> 1) & 1;
            const int v0 = rg * 8192 + ((c8 ^ (k0 << 1)) * 16), v1 = rg * 8192 + ((c8 ^ ((k0 | 2) << 1)) * 16);
            const int so = (128 * dcq + 32 * (w & 3)) * 8192 + dh * 256 + (w >> 2) * 128;
#pragma unroll
            for (int n = 0; n < 4; ++n) dma(sv, (n & 1) ? v1 : v0, so + n * 8 * 8192, L_WV + (w >> 2) * 16384 + ((w & 3) * 4 + n) * 1024);
        }
        {
            float mx = fmaxf(fmaxf(sacc[0], sacc[1]), fmaxf(sacc[2], sacc[3]));
            mx = max32(max16(mx));
            if (lane < 16) *(LAS float*)(L + L_MX + (i16 * 8 + w) * 4) = mx;
        }
        STAMP(4);
        BAR();
        {
            f32x4 m0 = *(LAS f32x4*)(L + L_MX + i16 * 32), m1 = *(LAS f32x4*)(L + L_MX + i16 * 32 + 16);
            float m = fmaxf(fmaxf(fmaxf(m0[0], m0[1]), fmaxf(m0[2], m0[3])), fmaxf(fmaxf(m1[0], m1[1]), fmaxf(m1[2], m1[3])));
            float p0 = __builtin_amdgcn_exp2f(sacc[0] - m), p1 = __builtin_amdgcn_exp2f(sacc[1] - m);
            float p2 = __builtin_amdgcn_exp2f(sacc[2] - m), p3 = __builtin_amdgcn_exp2f(sacc[3] - m);
            *(LAS v2u*)(L + L_P + i16 * 272 + (16 * w + 4 * g) * 2) = v2u{pk2(p0, p1), pk2(p2, p3)};
            BAR();
            if (w == 0) st_c1(ws, lane < 16 ? SM_OFF + ((b * 32 + kb) * 32 + 16 * hg + i16) * 4 : OOB, asu(m));
            v4u pf[4];
#pragma unroll
            for (int sg = 0; sg < 4; ++sg) pf[sg] = *(LAS v4u*)(L + L_P + i16 * 272 + (32 * sg + 8 * g) * 2);
            const int ob = OP_OFF + ((((b * 32 + 16 * hg + i16) * 4 + (w >> 1)) * 32 + kb) * 128 + 64 * (w & 1) + 8 * g) * 2;
#pragma unroll
            for (int u = 0; u < 2; ++u) {
                f32x4 o0 = Z4, o1 = Z4;
#pragma unroll
                for (int sg = 0; sg < 4; ++sg) {
                    o0 = mfma32(of[(2 * u) * 4 + sg], pf[sg], o0);
                    o1 = mfma32(of[(2 * u + 1) * 4 + sg], pf[sg], o1);
                }
                st_c(ws, ob + 64 * u, v4u{san(pk2(o0[0], o0[1])), san(pk2(o0[2], o0[3])), san(pk2(o1[0], o1[1])), san(pk2(o1[2], o1[3]))});
            }
            if (w == 7) {
                const v4u ones = {0x3F803F80u, 0x3F803F80u, 0x3F803F80u, 0x3F803F80u};
                f32x4 l = Z4;
#pragma unroll
                for (int sg = 0; sg < 4; ++sg) l = mfma32(ones, pf[sg], l);
                st_c1(ws, lane < 16 ? SL_OFF + ((b * 32 + kb) * 32 + 16 * hg + i16) * 4 : OOB, asu(l[0]));
            }
        }
        STAMP(5);
        if (w == 6) check_flags(ws, want, lane);
        VMW(0);
        BAR();
        v4u vf[16];
        if (w < 4) {
            const int kbl = 2 * ((i16 + 4 * w) & 15) + (g & 1);
            const int so = ((g >> 1) * 32 + kbl) * 128 + dh * 4;
            unsigned mu;
            for (unsigned s = 0; s < SPIN_CAP; ++s) {
                CBAR();
                mu = ld_c1(ws, SM_OFF + so);
                if (__all(mu != SENT)) break;
            }
            const float mv = asf(mu);
            const float M = max16(rowmax(mv));
            const float wf = __builtin_amdgcn_exp2f(mv - M);
            const unsigned wb = pk2(wf, wf);
            const unsigned keep = ((i16 >> 3) == (g >> 1)) ? hm : 0u;
            v4u af[4];
            {
                unsigned d0 = dppu<DPP_BC + 0>(wb) & keep, d1 = dppu<DPP_BC + 1>(wb) & keep, d2 = dppu<DPP_BC + 2>(wb) & keep, d3 = dppu<DPP_BC + 3>(wb) & keep;
#pragma unroll
                for (int q = 0; q < 4; ++q) {
                    af[0][q] = hq == q ? d0 : 0u; af[1][q] = hq == q ? d1 : 0u; af[2][q] = hq == q ? d2 : 0u; af[3][q] = hq == q ? d3 : 0u;
                }
                PIN(af[0]); PIN(af[1]); PIN(af[2]); PIN(af[3]);
            }
            STAMP(6);
            v4u o[4];
            unsigned lu;
            const int oo = OP_OFF + ((((g >> 1) * 32 + dh) * 4 + dcq) * 32 + 8 * w + (g & 1)) * 256 + i16 * 16;
            for (unsigned s = 0; s < SPIN_CAP; ++s) {
                CBAR();
#pragma unroll
                for (int j = 0; j < 4; ++j) o[j] = ld_c(ws, oo + j * 512);
                lu = ld_c1(ws, SL_OFF + so);
                if (__all(umax(umax(umax(mx4(o[0]), mx4(o[1])), umax(mx4(o[2]), mx4(o[3]))), lu) != SENT)) break;
            }
            STAMP(7);
            f32x4 d = Z4;
#pragma unroll
            for (int j = 0; j < 4; ++j) d = mfma32(af[j], o[j], d);
            const float Ls = add16(rowsum(bflo(wb) * asf(lu)));
            d *= __builtin_amdgcn_rcpf(Ls);
            const unsigned h0 = pk2(d[0], d[1]), h1 = pk2(d[2], d[3]);
            const unsigned l0 = pk2(d[0] - bflo(h0), d[1] - bfhi(h0)), l1 = pk2(d[2] - bflo(h1), d[3] - bfhi(h1));
            LAS unsigned char* p = L + L_CL + ((g >> 1) * 8 + 2 * w) * 272 + (8 * i16 + 4 * (g & 1)) * 2;
            *(LAS v2u*)p = v2u{h0, h1};
            *(LAS v2u*)(p + 272) = v2u{l0, l1};
        } else if (w < 6) {
            const int key = ((tq >> 1) & 1) | ((g & 1) << 1);
            LAS unsigned char* tb = L + L_WV + (w - 4) * 16384 + (8 * g + tq) * 128 + tp * 8;
#pragma unroll
            for (int tau = 0; tau < 4; ++tau)
#pragma unroll
                for (int s = 0; s < 4; ++s) {
                    LAS unsigned char* p = tb + ((tau ^ key) * 32) + s * 4096;
                    vf[tau * 4 + s] = trd(p, p + 512);
                }
        }
        BAR();
        if (w >= 4 && w < 6) {
            v4u af[4];
#pragma unroll
            for (int s = 0; s < 4; ++s) af[s] = *(LAS v4u*)(L + L_CL + i16 * 272 + (32 * s + 8 * g) * 2);
            f32x4 d[4] = {Z4, Z4, Z4, Z4};
#pragma unroll
            for (int s = 0; s < 4; ++s)
#pragma unroll
                for (int tau = 0; tau < 4; ++tau) d[tau] = mfma32(af[s], vf[tau * 4 + s], d[tau]);
            const float e0 = rs16(sum4(d[0]), sum4(d[2])), e1 = rs16(sum4(d[1]), sum4(d[3]));
            const unsigned hi = pk2(e0, e1), lo = pk2(e0 - bflo(hi), e1 - bfhi(hi));
            const int co = CX_OFF + dh * 4096 + ((g >> 1) * 8 + dcq * 2) * 256 + (32 * (w - 4) + 16 * (g & 1) + i16) * 4;
            st_c1(ws, co, san(hi));
            st_c1(ws, co + 256, san(lo));
        }
        STAMP(8);
        DUMP();
        return;
    }

    const int ai = r - 16, h = 4 * xg + (ai >> 2), part = ai & 3;
    const bool isF = ai < 9;
    const int fu = 9 * xg + ai;
    const int e = ai - 9, kg = xg >> 1, et = (xg & 1) ? 5 + e : e;
    const bool isE = !isF && (e < ((xg & 1) ? 4 : 5));
    v2u want = {0u, 0u};
    if (w >= 4) want = ld_c2(ws, EPOCH_OFF);
    if (w == 7) {
        st_c(ws, QP_OFF + h * 4096 + ((g >> 1) * 8 + part * 2 + (g & 1)) * 256 + i16 * 16, SENT4);
        st_c(ws, lane < 32 ? QA_OFF + ((g * 32 + h) * 512 + 128 * part + 8 * i16) * 2 : OOB, SENT4);
        if (isE) st_c(ws, lane < 32 ? AO_OFF + ((rg >> 1) * 8 + kg * 2 + (rg & 1)) * 1152 + et * 128 + c8 * 16 : OOB, SENT4);
        if (isF) {
#pragma unroll
            for (int i = 0; i < 5; ++i) st_c(ws, i * 64 + lane < 288 ? FO_OFF + fu * 4608 + (i * 64 + lane) * 16 : OOB, SENT4);
        }
        VMW(0);
        want = next_epoch(want);
        if (lane == 0) st_c2(ws, FL_OFF + (xg * 32 + r) * 8, want);
        STAMP(1);
        BAR();
        BAR();
        f32x4 v = *(LAS f32x4*)(L + L_RA + lane * 16);
#pragma unroll
        for (int pp = 1; pp < 4; ++pp) v += *(LAS f32x4*)(L + L_RA + pp * 1024 + lane * 16);
        const unsigned h0 = pk2(v[0], v[1]), h1 = pk2(v[2], v[3]);
        const int qo = QP_OFF + h * 4096 + ((g >> 1) * 8 + part * 2) * 256 + (8 * i16 + 4 * (g & 1)) * 2;
        st_c2(ws, qo, v2u{san(h0), san(h1)});
        const unsigned l0 = pk2(v[0] - bflo(h0), v[1] - bfhi(h0)), l1 = pk2(v[2] - bflo(h1), v[3] - bfhi(h1));
        st_c2(ws, qo + 256, v2u{san(l0), san(l1)});
        STAMP(3);
    } else if (w < 4) {
        const int xo = (lane >> 5) * 1152 + (lane & 31) * 36;
        v4u x0 = __builtin_amdgcn_raw_buffer_load_b128(rx, xo, 0, 0), x1 = __builtin_amdgcn_raw_buffer_load_b128(rx, xo + 16, 0, 0);
        unsigned x2 = __builtin_amdgcn_raw_buffer_load_b32(rx, xo + 32, 0, 0);
        const int xb = (i16 >> 3) * 576 + 144 * part;
        v4u xr = __builtin_amdgcn_raw_buffer_load_b128(rx, (xb + 32 * w + 8 * g) * 2, 0, 0);
        unsigned xe = __builtin_amdgcn_raw_buffer_load_b16(rx, (xb + 128 + 4 * w + g) * 2, 0, 0);
        const rsrc_t rq = mk(a.wq, 576 * 4096 * 2);
        v4u wq[9];
        const int qo = (144 * part + 32 * w + 8 * g) * 8192 + h * 256 + i16 * 16;
#pragma unroll
        for (int j = 0; j < 8; ++j) wq[j] = ld_nt(rq, qo + j * 8192);
        wq[8] = ld_nt(rq, (144 * part + 128 + 4 * w + g) * 8192 + h * 256 + i16 * 16);
        __builtin_amdgcn_sched_barrier(0);
        BAR();
        float xv[18];
#pragma unroll
        for (int q = 0; q < 4; ++q) { xv[2 * q] = bflo(x0[q]); xv[2 * q + 1] = bfhi(x0[q]); xv[8 + 2 * q] = bflo(x1[q]); xv[9 + 2 * q] = bfhi(x1[q]); }
        xv[16] = bflo(x2); xv[17] = bfhi(x2);
        float s = 0.f;
#pragma unroll
        for (int q = 0; q < 18; ++q) s += xv[q];
        float mean = add16(rowsum(s)) * (1.0f / 576.0f);
        float vs = 0.f;
#pragma unroll
        for (int q = 0; q < 18; ++q) { float d = xv[q] - mean; vs += d * d; }
        float rs = __builtin_amdgcn_rsqf(add16(rowsum(vs)) * (1.0f / 576.0f) + 1e-5f) * (0.08838834764831845f * 1.4426950408889634f);
        v4u mk4, af[9], ones;
#pragma unroll
        for (int q = 0; q < 4; ++q) { mk4[q] = hq == q ? hm : 0u; ones[q] = mk4[q] & 0x3F803F80u; }
#pragma unroll
        for (int j = 0; j < 9; ++j) {
            const unsigned src = j < 8 ? xr[j >> 1] : xe;
            const unsigned dup = __builtin_amdgcn_perm(src, src, (j & 1) ? 0x03020302u : 0x01000100u);
#pragma unroll
            for (int q = 0; q < 4; ++q) af[j][q] = dup & mk4[q];
            PIN(af[j]);
        }
        PIN(ones); PIN(mean); PIN(rs);
        STAMP(1);
        f32x4 acc = Z4, cs = Z4;
#pragma unroll
        for (int j = 0; j < 9; ++j) { acc = mfma32(af[j], wq[j], acc); cs = mfma32(ones, wq[j], cs); }
        *(LAS f32x4*)(L + L_RA + w * 1024 + lane * 16) = (acc - cs * mean) * rs;
        STAMP(2);
        BAR();
    } else if (w < 6) {
        BAR();
        const int wb = w - 4;
        {
            const v4u sk = mks(a.wuk, 512 * 4096 * 2);
            const int so = (128 * part + 64 * wb) * 8192 + h * 256;
#pragma unroll
            for (int t = 0; t < 16; ++t) dma(sk, g * 8192 + ((i16 ^ t) * 16), so + t * 4 * 8192, L_UK + wb * 16384 + t * 1024);
        }
        want = next_epoch(want);
        const int fo = FL_OFF + (xg * 32 + 16 + (ai & ~3) + (lane & 3)) * 8;
        BAR();
        v2u fl = ld_c2(ws, fo);
        VMW(0);
        STAMP(4);
        v4u uk[16];
#pragma unroll
        for (int tau = 0; tau < 4; ++tau)
#pragma unroll
            for (int s = 0; s < 4; ++s) uk[tau * 4 + s] = *(LAS v4u*)(L + L_UK + wb * 16384 + i16 * 1024 + tau * 256 + (((4 * s + g) ^ i16) * 16));
        for (unsigned s = 0; s < SPIN_CAP; ++s) {
            if (__all(((fl.x ^ want.x) | (fl.y ^ want.y)) == 0u)) break;
            CBAR();
            fl = ld_c2(ws, fo);
        }
        v4u qf[4];
        const int po = QP_OFF + h * 4096 + i16 * 256 + g * 16;
        int qa = QA_OFF + (((g >> 1) * 32 + h) * 512 + 128 * part + 64 * wb + 4 * i16 + 2 * (g & 1)) * 2;
        PIN(qa);
        for (unsigned s = 0; s < SPIN_CAP; ++s) {
            CBAR();
#pragma unroll
            for (int j = 0; j < 4; ++j) qf[j] = ld_c(ws, po + j * 64);
            if (__all(umax(umax(mx4(qf[0]), mx4(qf[1])), umax(mx4(qf[2]), mx4(qf[3]))) != SENT)) break;
        }
        STAMP(5);
        f32x4 d[4] = {Z4, Z4, Z4, Z4};
#pragma unroll
        for (int s = 0; s < 4; ++s)
#pragma unroll
            for (int tau = 0; tau < 4; ++tau) d[tau] = mfma32(qf[s], uk[tau * 4 + s], d[tau]);
        const float e0 = rs16(sum4(d[0]), sum4(d[2])), e1 = rs16(sum4(d[1]), sum4(d[3]));
        st_c1(ws, qa, san(pk2(e0, e1)));
        STAMP(6);
    } else {
        want = next_epoch(want);
        BAR();
        BAR();
    }
    BAR();

    if (isF) {
        const v4u sg_ = mks(a.wg, 576 * 4608 * 2), su_ = mks(a.wu, 576 * 4608 * 2), sd_ = mks(a.wd, 4608 * 576 * 2);
        {
            const int key = ((rg >> 1) & 1) | ((w & 1) << 1);
            const int vd = rg * 1152 + ((c8 ^ (key << 1)) * 16);
#pragma unroll
            for (int bl = 0; bl < 9; ++bl) dma(sd_, vd, (64 * fu + 8 * w) * 1152 + bl * 128, L_DN + bl * 8192 + w * 1024);
        }
        const int keyg = (((w & 3) >> 1) & 1) | (((rg >> 2) & 1) << 1);
        const int vg = rg * 9216 + ((c8 ^ (keyg << 1)) * 16);
        const v4u sgu = (w >> 2) ? su_ : sg_;
        const int sgo = 8 * (w & 3) * 9216 + fu * 128;
#pragma unroll
        for (int s = 0; s < 10; ++s) dma(sgu, vg, sgo + s * 32 * 9216, L_GU1 + s * 8192 + w * 1024);
        VMW(10);
        BAR();
        v4u df[10];
        {
            const int key = ((tq >> 1) & 1) | ((g & 1) << 1);
            LAS unsigned char* tb = L + L_DN + (w >> 2) * 8192 + (8 * g + tq) * 128 + tp * 8 + (((w & 3) ^ key) * 32);
#pragma unroll
            for (int i = 0; i < 5; ++i)
#pragma unroll
                for (int s = 0; s < 2; ++s) {
                    LAS unsigned char* p = tb + (i < 4 ? i : (w < 4 ? 4 : 0)) * 16384 + s * 4096;
                    df[i * 2 + s] = trd(p, p + 512);
                }
        }
        BAR();
#pragma unroll
        for (int s = 10; s < 18; ++s) dma(sgu, vg, sgo + s * 32 * 9216, L_GU2 + (s - 10) * 8192 + w * 1024);
        VMW(8);
        BAR();
        v4u gf[18], uf[18];
        const int keyp = (tq & 1) | ((g & 1) << 1);
        const int tro = (16 * (tq & 1) + 4 * g + (tq >> 1)) * 128 + tp * 8 + (((w & 3) ^ keyp) * 32);
        if (w < 4) {
#pragma unroll
            for (int s = 0; s < 10; ++s) {
                LAS unsigned char* p = L + L_GU1 + s * 8192 + tro;
                gf[s] = trd(p, p + 256);
                uf[s] = trd(p + 4096, p + 4096 + 256);
            }
        }
        if (w == 6) check_flags(ws, want, lane);
        VMW(0);
        BAR();
        STAMP(7);
        const v4u ones = {0x3F803F80u, 0x3F803F80u, 0x3F803F80u, 0x3F803F80u};
        float csg = 0.f, csu = 0.f;
        if (w < 4) {
#pragma unroll
            for (int s = 10; s < 18; ++s) {
                LAS unsigned char* p = L + L_GU2 + (s - 10) * 8192 + tro;
                gf[s] = trd(p, p + 256);
                uf[s] = trd(p + 4096, p + 4096 + 256);
            }
            f32x4 cg = Z4, cu = Z4;
#pragma unroll
            for (int s = 0; s < 18; ++s) { cg = mfma32(ones, gf[s], cg); cu = mfma32(ones, uf[s], cu); }
            csg = cg[0]; csu = cu[0];
            PIN(csg); PIN(csu);
        } else {
            const int wq_ = w - 4;
            int go[5], lo_[5];
#pragma unroll
            for (int i = 0; i < 5; ++i) {
                const int id = i * 64 + lane;
                go[i] = id < 288 ? AO_OFF + wq_ * 4608 + id * 16 : OOB;
                lo_[i] = id < 288 ? L_AO + wq_ * 4 * AOP + id * 16 + (id / 72) * 16 : L_AO + 16 * AOP + lane * 16;
                PIN(lo_[i]);
            }
            v4u av[5];
            for (unsigned s = 0; s < SPIN_CAP; ++s) {
                CBAR();
#pragma unroll
                for (int i = 0; i < 5; ++i) av[i] = ld_c(ws, go[i]);
                if (__all(umax(umax(umax(mx4(av[0]), mx4(av[1])), umax(mx4(av[2]), mx4(av[3]))), mx4(av[4])) != SENT)) break;
            }
            STAMP(8);
#pragma unroll
            for (int i = 0; i < 5; ++i) *(LAS v4u*)(L + lo_[i]) = av[i];
        }
        BAR();
        LAS unsigned char* ab = L + L_AO + i16 * AOP + g * 16;
        f32x4 dg = Z4, du = Z4;
        if (w < 4) {
#pragma unroll
            for (int s = 0; s < 18; ++s) {
                v4u af = *(LAS v4u*)(ab + s * 64);
                dg = mfma32(af, gf[s], dg);
                du = mfma32(af, uf[s], du);
            }
        } else {
            const int wq_ = w - 4;
#pragma unroll
            for (int i = 0; i < 5; ++i) {
                if (i < 4 || wq_ < 2) {
                    v4u af = *(LAS v4u*)(ab + (wq_ + 4 * i) * 64);
                    dg = mfma32(af, af, dg);
                    du = mfma32(af, ones, du);
                }
            }
            float s2 = ((i16 >> 3) == (g >> 1)) ? sum4(dg) : 0.f;
            s2 = rowsum(s2);
            const float st = rs16(sum4(du), s2);
            if (i16 == 0) *(LAS float*)(L + L_FS + (g * 4 + wq_) * 4) = st;
        }
        BAR();
        if (w < 4) {
            const f32x4 s1 = *(LAS f32x4*)(L + L_FS + (g >> 1) * 32), s2 = *(LAS f32x4*)(L + L_FS + (g >> 1) * 32 + 16);
            const float gs = add16(sum4(dg)), us = add16(sum4(du));
            const float mean = sum4(s1) * (1.0f / 576.0f);
            const float var = fmaxf(sum4(s2) * (1.0f / 576.0f) - mean * mean, 0.f);
            const float rstd = __builtin_amdgcn_rsqf(var + 1e-5f);
            const float gg = (gs - mean * csg) * rstd, uu = (us - mean * csu) * rstd;
            const float act = gg * uu * __builtin_amdgcn_rcpf(1.0f + __builtin_amdgcn_exp2f(gg * -1.4426950408889634f));
            if ((g & 1) == 0) *(LAS unsigned short*)(L + L_ACT + (g >> 1) * 128 + (16 * w + i16) * 2) = (unsigned short)pk2(act, act);
        }
        BAR();
        {
            const v4u b0 = *(LAS v4u*)(L + L_ACT + (i16 & 1) * 128 + g * 16), b1 = *(LAS v4u*)(L + L_ACT + (i16 & 1) * 128 + 64 + g * 16);
            const int fo = i16 < 2 ? FO_OFF + fu * 4608 + w * 128 + i16 * 64 + g * 16 : OOB;
#pragma unroll
            for (int i = 0; i < 5; ++i) {
                if (i < 4 || w < 4) {
                    f32x4 d = mfma32(df[2 * i + 1], b1, mfma32(df[2 * i], b0, Z4));
                    st_c(ws, fo + i * 1024, __builtin_bit_cast(v4u, d));
                }
            }
        }
        STAMP(9);
        DUMP();
        return;
    }
    if (!isE) {
        if (blockIdx.x == 255 && w == 6) {
            check_flags(ws, want, lane);
            if (lane == 0) st_c2(ws, EPOCH_OFF, want);
        }
        DUMP();
        return;
    }
    {
        const int hh = 8 * kg + w;
        {
            const v4u so_ = mks(a.wo, 4096 * 576 * 2);
            const int k1 = ((rg >> 2) & 1) << 1;
            const int v0 = rg * 1152 + ((c8 ^ (k1 << 1)) * 16), v1 = rg * 1152 + ((c8 ^ ((k1 | 1) << 1)) * 16);
#pragma unroll
            for (int n = 0; n < 16; ++n) dma(so_, ((n >> 1) & 1) ? v1 : v0, (hh * 128 + 8 * n) * 1152 + et * 128, L_WO + w * 16384 + n * 1024);
        }
        const int xc = (g >> 1) * 576 + 64 * et + 32 * (g & 1) + i16;
        unsigned xa = __builtin_amdgcn_raw_buffer_load_b16(rx, kg == 0 ? xc * 2 : OOB, 0, 0);
        unsigned xb2 = __builtin_amdgcn_raw_buffer_load_b16(rx, kg == 0 ? (xc + 16) * 2 : OOB, 0, 0);
        if (w == 6) check_flags(ws, want, lane);
        VMW(0);
        v4u wf[16];
        {
            const int keyp = (tq & 1) | ((g & 1) << 1);
            LAS unsigned char* tb = L + L_WO + w * 16384 + (16 * (tq & 1) + 4 * g + (tq >> 1)) * 128 + tp * 8;
#pragma unroll
            for (int tau = 0; tau < 4; ++tau)
#pragma unroll
                for (int s = 0; s < 4; ++s) {
                    LAS unsigned char* p = tb + ((tau ^ keyp) * 32) + s * 4096;
                    wf[tau * 4 + s] = trd(p, p + 256);
                }
        }
        BAR();
        STAMP(7);
        v4u cf[4];
        const int co = CX_OFF + hh * 4096 + i16 * 256 + g * 16;
        for (unsigned s = 0; s < SPIN_CAP; ++s) {
            CBAR();
#pragma unroll
            for (int j = 0; j < 4; ++j) cf[j] = ld_c(ws, co + j * 64);
            if (__all(umax(umax(mx4(cf[0]), mx4(cf[1])), umax(mx4(cf[2]), mx4(cf[3]))) != SENT)) break;
        }
        STAMP(8);
        {
            f32x4 d[4] = {Z4, Z4, Z4, Z4};
#pragma unroll
            for (int s = 0; s < 4; ++s)
#pragma unroll
                for (int tau = 0; tau < 4; ++tau) d[tau] = mfma32(cf[s], wf[tau * 4 + s], d[tau]);
            const float e0 = rs16(sum4(d[0]), sum4(d[2])), e1 = rs16(sum4(d[1]), sum4(d[3]));
            *(LAS f32x2*)(L + L_RE + w * 512 + lane * 8) = f32x2{e0, e1};
        }
        BAR();
        const int ru = 9 * kg + et;
        if (w == 0) {
            f32x2 v = {bflo(xa), bflo(xb2)};
#pragma unroll
            for (int pp = 0; pp < 8; ++pp) v += *(LAS f32x2*)(L + L_RE + pp * 512 + lane * 8);
            const unsigned hi = pk2(v[0], v[1]), lo = pk2(v[0] - bflo(hi), v[1] - bfhi(hi));
            const int ao = AO_OFF + ((g >> 1) * 8 + kg * 2) * 1152 + et * 128 + (16 * (g & 1) + i16) * 4;
            st_c1(ws, ao, san(hi));
            st_c1(ws, ao + 1152, san(lo));
            STAMP(9);
        } else if (w == 1) {
            v4u hv;
            const int ho = AO_OFF + ((c8 >> 2) * 8 + rg) * 1152 + (ru >> 1) * 64 + (c8 & 3) * 16;
            for (unsigned s = 0; s < SPIN_CAP; ++s) {
                CBAR();
                hv = ld_c(ws, ho);
                if (__all(mx4(hv) != SENT)) break;
            }
            f32x4 acc;
#pragma unroll
            for (int j = 0; j < 4; ++j) acc[j] = (ru & 1) ? bfhi(hv[j]) : bflo(hv[j]);
            PIN(acc);
            v4u fv[9];
            const int fo = FO_OFF + rg * 4608 + ru * 128 + c8 * 16;
            for (unsigned s = 0; s < SPIN_CAP; ++s) {
                CBAR();
#pragma unroll
                for (int j = 0; j < 9; ++j) fv[j] = ld_c(ws, fo + j * 8 * 4608);
                unsigned m = mx4(fv[0]);
#pragma unroll
                for (int j = 1; j < 9; ++j) m = umax(m, mx4(fv[j]));
                if (__all(m != SENT)) break;
            }
            STAMP(10);
#pragma unroll
            for (int j = 0; j < 9; ++j) acc += __builtin_bit_cast(f32x4, fv[j]);
            float u0 = rs32(acc[0], acc[2]), u1 = rs32(acc[1], acc[3]);
            u0 = add16(u0); u1 = add16(u1);
            u0 += dpp<DPP_ROR8>(u0); u1 += dpp<DPP_ROR8>(u1);
            if ((lane & 31) < 8)
                *(unsigned*)((unsigned char*)a.out + ((c8 >> 2) * 576 + 16 * ru + 4 * (c8 & 3) + 2 * (lane >> 5)) * 2) = pk2(u0, u1);
            STAMP(11);
        }
        DUMP();
    }
}

}

extern "C" void kernel_launch(void* const* d_in, const int* in_sizes, int n_in, void* d_out, int out_size, void* d_ws,
                              size_t ws_size, hipStream_t stream) {
    Args a;
    a.x = d_in[0]; a.ckv = d_in[1]; a.wq = d_in[2]; a.wuk = d_in[3]; a.wuv = d_in[4]; a.wo = d_in[5];
    a.wg = d_in[7]; a.wu = d_in[8]; a.wd = d_in[9];
    a.out = d_out; a.ws = (unsigned char*)d_ws;
    (void)hipFuncSetAttribute((const void*)fused, hipFuncAttributeMaxDynamicSharedMemorySize, LDS_BYTES);
    hipLaunchKernelGGL(fused, dim3(256), dim3(NT), LDS_BYTES, stream, a);
}
```

```cpp
#define STAMPS 1
#define PROBE_G 0
#define PROBE_I 0
#include <hip/hip_runtime.h>
#include <hip/hip_bf16.h>

#define LAS __attribute__((address_space(3)))
typedef unsigned v4u __attribute__((ext_vector_type(4)));
typedef unsigned v2u __attribute__((ext_vector_type(2)));
typedef float f32x4 __attribute__((ext_vector_type(4)));
typedef float f32x2 __attribute__((ext_vector_type(2)));
typedef short s16x4 __attribute__((ext_vector_type(4)));
typedef __bf16 bf16x8 __attribute__((ext_vector_type(8)));
typedef __bf16 bf16x2 __attribute__((ext_vector_type(2)));
typedef __amdgpu_buffer_rsrc_t rsrc_t;
#define DI __device__ __forceinline__

#pragma clang diagnostic ignored "-Winline-asm"
#define KVER 2
namespace {

constexpr int NT = 512, NW = 8;
constexpr unsigned SENT = 0xFFFFFFFFu;
constexpr int AUX_NT = 2, AUX_SC1 = 16;
constexpr unsigned SPIN_CAP = 1u << 18;
constexpr int OOB = 0x7ffff000;

constexpr int EPOCH_OFF = 0;
constexpr int FL_OFF = 1024;
constexpr int QP_OFF = 4096;
constexpr int QA_OFF = QP_OFF + 131072;
constexpr int SM_OFF = QA_OFF + 65536;
constexpr int SL_OFF = SM_OFF + 8192;
constexpr int CX_OFF = SL_OFF + 8192;
constexpr int AO_OFF = CX_OFF + 131072;
constexpr int FO_OFF = AO_OFF + 18432;
constexpr int OP_OFF = 1 << 20;
constexpr int WS_BYTES = OP_OFF + (2 << 20);
constexpr int DBG_OFF = 8 << 20;

constexpr int CKP = 1040;
constexpr int L_CK = 0;
constexpr int L_Q = 128 * CKP;
constexpr int L_P = L_Q + 16 * CKP;
constexpr int L_MX = L_P + 16 * 272;
constexpr int L_WV = 0;
constexpr int L_CL = 40960;
constexpr int L_UK = 0;
constexpr int L_RA = 32768;
constexpr int L_DN = 0;
constexpr int L_GU1 = 73728;
constexpr int L_GU2 = 0;
constexpr int L_AO = 73728;
constexpr int AOP = 1168;
constexpr int L_FS = 155648;
constexpr int L_ACT = L_FS + 64;
constexpr int L_WO = 0;
constexpr int L_RE = 131072;
constexpr int L_DBG = 156672;
constexpr int LDS_BYTES = L_DBG + 8 * 16 * 8;
static_assert(L_MX + 512 <= L_DBG && L_ACT + 256 <= L_DBG, "lds map");

DI v4u ld_nt(rsrc_t r, int off) { return __builtin_amdgcn_raw_buffer_load_b128(r, off, 0, AUX_NT); }
DI v4u ld_c(rsrc_t r, int off) { return __builtin_amdgcn_raw_buffer_load_b128(r, off, 0, AUX_SC1); }
DI v2u ld_c2(rsrc_t r, int off) { return __builtin_amdgcn_raw_buffer_load_b64(r, off, 0, AUX_SC1); }
DI unsigned ld_c1(rsrc_t r, int off) { return __builtin_amdgcn_raw_buffer_load_b32(r, off, 0, AUX_SC1); }
DI void st_c(rsrc_t r, int off, v4u v) { __builtin_amdgcn_raw_buffer_store_b128(v, r, off, 0, AUX_SC1); }
DI void st_c2(rsrc_t r, int off, v2u v) { __builtin_amdgcn_raw_buffer_store_b64(v, r, off, 0, AUX_SC1); }
DI void st_c1(rsrc_t r, int off, unsigned v) { __builtin_amdgcn_raw_buffer_store_b32(v, r, off, 0, AUX_SC1); }
DI unsigned san(unsigned x) { return x < 0xFFFFFFFEu ? x : 0xFFFFFFFEu; }
DI rsrc_t mk(const void* p, unsigned bytes) { return __builtin_amdgcn_make_buffer_rsrc((void*)p, 0, bytes, 0x00020000); }
DI v4u mks(const void* p, unsigned bytes) {
    const unsigned long long a = (unsigned long long)p;
    return v4u{(unsigned)a, (unsigned)(a >> 32) & 0xFFFFu, bytes, 0x00020000u};
}
DI void dma(v4u srd, int voff, int soff, int lds) {
    asm volatile("s_mov_b32 m0, %0\n\ts_nop 0\n\tbuffer_load_dwordx4 %1, %2, %3 offen nt lds" ::"s"(lds), "v"(voff), "s"(srd), "s"(soff) : "m0", "memory");
}
DI float asf(unsigned u) { return __builtin_bit_cast(float, u); }
DI unsigned asu(float f) { return __builtin_bit_cast(unsigned, f); }
DI float bflo(unsigned u) { return asf(u << 16); }
DI float bfhi(unsigned u) { return asf(u & 0xFFFF0000u); }
DI unsigned pk2(float a, float b) { bf16x2 t = {(__bf16)a, (__bf16)b}; return __builtin_bit_cast(unsigned, t); }
DI f32x4 mfma32(v4u a, v4u b, f32x4 c) {
    return __builtin_amdgcn_mfma_f32_16x16x32_bf16(__builtin_bit_cast(bf16x8, a), __builtin_bit_cast(bf16x8, b), c, 0, 0, 0);
}
template <int CTRL> DI float dpp(float v) { return asf((unsigned)__builtin_amdgcn_update_dpp(0, (int)asu(v), CTRL, 0xf, 0xf, true)); }
template <int CTRL> DI unsigned dppu(unsigned v) { return (unsigned)__builtin_amdgcn_update_dpp(0, (int)v, CTRL, 0xf, 0xf, true); }
constexpr int DPP_X1 = 0xB1, DPP_X2 = 0x4E, DPP_HMIR = 0x141, DPP_ROR8 = 0x128, DPP_ROR4 = 0x124, DPP_ROR2 = 0x122, DPP_ROR1 = 0x121;
constexpr int DPP_BC = 0x150;
DI float rs16(float a, float b) { auto r = __builtin_amdgcn_permlane16_swap(asu(a), asu(b), false, false); unsigned x = r[0], y = r[1]; return asf(x) + asf(y); }
DI float rs32(float a, float b) { auto r = __builtin_amdgcn_permlane32_swap(asu(a), asu(b), false, false); unsigned x = r[0], y = r[1]; return asf(x) + asf(y); }
DI float add16(float v) { return rs16(v, v); }
DI float max16(float v) { auto r = __builtin_amdgcn_permlane16_swap(asu(v), asu(v), false, false); unsigned a = r[0], b = r[1]; return fmaxf(asf(a), asf(b)); }
DI float max32(float v) { auto r = __builtin_amdgcn_permlane32_swap(asu(v), asu(v), false, false); unsigned a = r[0], b = r[1]; return fmaxf(asf(a), asf(b)); }
DI float rowsum(float v) { v += dpp<DPP_ROR8>(v); v += dpp<DPP_ROR4>(v); v += dpp<DPP_ROR2>(v); v += dpp<DPP_ROR1>(v); return v; }
DI float rowmax(float v) { v = fmaxf(v, dpp<DPP_ROR8>(v)); v = fmaxf(v, dpp<DPP_ROR4>(v)); v = fmaxf(v, dpp<DPP_ROR2>(v)); v = fmaxf(v, dpp<DPP_ROR1>(v)); return v; }
DI float sum4(f32x4 d) { return (d[0] + d[2]) + (d[1] + d[3]); }
DI unsigned umax(unsigned a, unsigned b) { return a > b ? a : b; }
DI unsigned mx4(v4u v) { return umax(umax(v.x, v.y), umax(v.z, v.w)); }
DI v4u trd(LAS unsigned char* p0, LAS unsigned char* p1) {
    v2u a = __builtin_bit_cast(v2u, __builtin_amdgcn_ds_read_tr16_b64_v4i16((LAS s16x4*)p0));
    v2u b = __builtin_bit_cast(v2u, __builtin_amdgcn_ds_read_tr16_b64_v4i16((LAS s16x4*)p1));
    return v4u{a.x, a.y, b.x, b.y};
}
#define CBAR() asm volatile("" ::: "memory")
#define VMW(n) asm volatile("s_waitcnt vmcnt(" #n ")" ::: "memory")
#define BAR() asm volatile("s_waitcnt lgkmcnt(0)\n\ts_barrier" ::: "memory")
#define PIN(v) asm volatile("" : "+v"(v))
constexpr f32x4 Z4 = {0.f, 0.f, 0.f, 0.f};

DI void check_flags(rsrc_t ws, v2u want, int lane) {
    for (unsigned s = 0; s < SPIN_CAP; ++s) {
        CBAR();
        v4u a = ld_c(ws, FL_OFF + lane * 16), b = ld_c(ws, FL_OFF + 1024 + lane * 16);
        unsigned d = (a.x ^ want.x) | (a.y ^ want.y) | (a.z ^ want.x) | (a.w ^ want.y) | (b.x ^ want.x) | (b.y ^ want.y) | (b.z ^ want.x) | (b.w ^ want.y);
        if (__all(d == 0u)) break;
    }
    CBAR();
}
DI v2u next_epoch(v2u ep) {
    unsigned long long e = (((unsigned long long)ep.y << 32) | ep.x) + 1ull;
    return v2u{(unsigned)e, (unsigned)(e >> 32)};
}

#define STAMP(i) do { unsigned long long t_; __builtin_amdgcn_sched_barrier(0); asm volatile("s_memrealtime %0\n\ts_waitcnt lgkmcnt(0)" : "=s"(t_) :: "memory"); \
    if (lane == 0) *(LAS unsigned long long*)(L + L_DBG + (w * 16 + (i)) * 8) = t_; __builtin_amdgcn_sched_barrier(0); } while (0)
#define DUMP() do { const int gg_ = r < 16 ? 0 : (r < 25 ? 1 : 2); if (gg_ == PROBE_G) { asm volatile("s_waitcnt lgkmcnt(0)" ::: "memory"); \
    unsigned long long ta_ = *(LAS unsigned long long*)(L + L_DBG + (w * 16) * 8), tb_ = *(LAS unsigned long long*)(L + L_DBG + (w * 16 + PROBE_I) * 8); \
    if (tb_) { unsigned long long te_ = ta_ + 4000ull + 10ull * (tb_ - ta_); while (__builtin_amdgcn_s_memrealtime() < te_) __builtin_amdgcn_s_sleep(2); } } } while (0)
#define STAMP_INIT() do { if (lane < 16) *(LAS unsigned long long*)(L + L_DBG + (w * 16 + lane) * 8) = 0ull; } while (0)

struct Args {
    const void *x, *ckv, *wq, *wuk, *wuv, *wo, *wg, *wu, *wd;
    void* out;
    unsigned char* ws;
};

__global__ void __launch_bounds__(NT, 2) fused(Args a) {
    extern __shared__ __attribute__((aligned(16))) unsigned char smem[];
    LAS unsigned char* L = (LAS unsigned char*)smem;
    const int tid = threadIdx.x, lane = tid & 63, w = __builtin_amdgcn_readfirstlane(tid >> 6);
    const int xg = blockIdx.x & 7, r = blockIdx.x >> 3;
    const rsrc_t ws = mk(a.ws, WS_BYTES);
    const rsrc_t rx = mk(a.x, 2 * 576 * 2);
    const int c8 = lane & 7, rg = lane >> 3, i16 = lane & 15, g = lane >> 4;
    const int tq = i16 >> 2, tp = i16 & 3;
    const v4u SENT4 = {SENT, SENT, SENT, SENT};
    const unsigned hm = (i16 & 1) ? 0xFFFF0000u : 0x0000FFFFu;
    const int hq = (i16 >> 1) & 3;
    STAMP_INIT();
    STAMP(0);

    if (r < 16) {
        const int b = xg >> 2, kb = 8 * (xg & 3) + (r >> 1), hg = r & 1;
        const int dh = 4 * xg + (r >> 2), dcq = r & 3;
#pragma unroll
        for (int i = 0; i < 2; ++i)
            st_c(ws, OP_OFF + ((((b * 32 + 16 * hg + 2 * w + i) * 4 + g) * 32 + kb) * 256) + i16 * 16, SENT4);
        if (w == 0) {
            st_c(ws, lane < 8 ? SM_OFF + (lane >> 2) * 8192 + ((b * 32 + kb) * 32 + 16 * hg) * 4 + (lane & 3) * 16 : OOB, SENT4);
            st_c(ws, CX_OFF + dh * 4096 + ((g >> 1) * 8 + dcq * 2 + (g & 1)) * 256 + i16 * 16, SENT4);
        }
        const v2u want = next_epoch(ld_c2(ws, EPOCH_OFF));
        {
            const v4u sc = mks(a.ckv, 2 * 4096 * 512 * 2);
            const int so = (b * 4096 + 128 * kb + 16 * w) * 1024;
#pragma unroll
            for (int i = 0; i < 12; ++i) dma(sc, lane * 16, so + i * 1024, L_CK + (16 * w + i) * CKP);
            VMW(6);
            BAR();
            if (w == 0 && lane == 0) st_c2(ws, FL_OFF + (xg * 32 + r) * 8, want);
            v2u fl = ld_c2(ws, FL_OFF + (w * 32 + 16 + i16) * 8);
#pragma unroll
            for (int i = 12; i < 16; ++i) dma(sc, lane * 16, so + i * 1024, L_CK + (16 * w + i) * CKP);
            VMW(0);
            STAMP(1);
            for (unsigned s = 0; s < SPIN_CAP; ++s) {
                if (__all(((fl.x ^ want.x) | (fl.y ^ want.y)) == 0u)) break;
                CBAR();
                fl = ld_c2(ws, FL_OFF + (w * 32 + 16 + i16) * 8);
            }
        }
        v4u sf[16], of[16];
#pragma unroll
        for (int s = 0; s < 16; ++s) sf[s] = *(LAS v4u*)(L + L_CK + (16 * w + i16) * CKP + (4 * s + g) * 16);
        BAR();
        const int qoff = QA_OFF + (b * 32 + 16 * hg) * 1024 + tid * 16;
        v4u q0 = ld_c(ws, qoff), q1 = ld_c(ws, qoff + 8192);
        {
            LAS unsigned char* tb = L + L_CK + (8 * g + tq) * CKP + (64 * w + 8 * tp) * 2;
#pragma unroll
            for (int tau = 0; tau < 4; ++tau)
#pragma unroll
                for (int sg = 0; sg < 4; ++sg) {
                    LAS unsigned char* p = tb + sg * 32 * CKP + (32 * (tau >> 1) + 4 * (tau & 1)) * 2;
                    of[tau * 4 + sg] = trd(p, p + 4 * CKP);
                }
        }
        STAMP(2);
        for (unsigned s = 0; s < SPIN_CAP; ++s) {
            if (__all(umax(mx4(q0), mx4(q1)) != SENT)) break;
            CBAR();
            q0 = ld_c(ws, qoff); q1 = ld_c(ws, qoff + 8192);
        }
        STAMP(3);
        *(LAS v4u*)(L + L_Q + w * CKP + lane * 16) = q0;
        *(LAS v4u*)(L + L_Q + (8 + w) * CKP + lane * 16) = q1;
        BAR();
        f32x4 sacc = Z4;
#pragma unroll
        for (int s = 0; s < 16; ++s) {
            v4u qf = *(LAS v4u*)(L + L_Q + i16 * CKP + (4 * s + g) * 16);
            sacc = mfma32(sf[s], qf, sacc);
        }
        {
            const v4u sv = mks(a.wuv, 512 * 4096 * 2);
            const int k0 = (rg >> 1) & 1;
            const int v0 = rg * 8192 + ((c8 ^ (k0 << 1)) * 16), v1 = rg * 8192 + ((c8 ^ ((k0 | 2) << 1)) * 16);
            const int so = (128 * dcq + 32 * (w & 3)) * 8192 + dh * 256 + (w >> 2) * 128;
#pragma unroll
            for (int n = 0; n < 4; ++n) dma(sv, (n & 1) ? v1 : v0, so + n * 8 * 8192, L_WV + (w >> 2) * 16384 + ((w & 3) * 4 + n) * 1024);
        }
        {
            float mx = fmaxf(fmaxf(sacc[0], sacc[1]), fmaxf(sacc[2], sacc[3]));
            mx = max32(max16(mx));
            if (lane < 16) *(LAS float*)(L + L_MX + (i16 * 8 + w) * 4) = mx;
        }
        STAMP(4);
        BAR();
        {
            f32x4 m0 = *(LAS f32x4*)(L + L_MX + i16 * 32), m1 = *(LAS f32x4*)(L + L_MX + i16 * 32 + 16);
            float m = fmaxf(fmaxf(fmaxf(m0[0], m0[1]), fmaxf(m0[2], m0[3])), fmaxf(fmaxf(m1[0], m1[1]), fmaxf(m1[2], m1[3])));
            float p0 = __builtin_amdgcn_exp2f(sacc[0] - m), p1 = __builtin_amdgcn_exp2f(sacc[1] - m);
            float p2 = __builtin_amdgcn_exp2f(sacc[2] - m), p3 = __builtin_amdgcn_exp2f(sacc[3] - m);
            *(LAS v2u*)(L + L_P + i16 * 272 + (16 * w + 4 * g) * 2) = v2u{pk2(p0, p1), pk2(p2, p3)};
            BAR();
            if (w == 0) st_c1(ws, lane < 16 ? SM_OFF + ((b * 32 + kb) * 32 + 16 * hg + i16) * 4 : OOB, asu(m));
            v4u pf[4];
#pragma unroll
            for (int sg = 0; sg < 4; ++sg) pf[sg] = *(LAS v4u*)(L + L_P + i16 * 272 + (32 * sg + 8 * g) * 2);
            const int ob = OP_OFF + ((((b * 32 + 16 * hg + i16) * 4 + (w >> 1)) * 32 + kb) * 128 + 64 * (w & 1) + 8 * g) * 2;
#pragma unroll
            for (int u = 0; u < 2; ++u) {
                f32x4 o0 = Z4, o1 = Z4;
#pragma unroll
                for (int sg = 0; sg < 4; ++sg) {
                    o0 = mfma32(of[(2 * u) * 4 + sg], pf[sg], o0);
                    o1 = mfma32(of[(2 * u + 1) * 4 + sg], pf[sg], o1);
                }
                st_c(ws, ob + 64 * u, v4u{san(pk2(o0[0], o0[1])), san(pk2(o0[2], o0[3])), san(pk2(o1[0], o1[1])), san(pk2(o1[2], o1[3]))});
            }
            if (w == 7) {
                const v4u ones = {0x3F803F80u, 0x3F803F80u, 0x3F803F80u, 0x3F803F80u};
                f32x4 l = Z4;
#pragma unroll
                for (int sg = 0; sg < 4; ++sg) l = mfma32(ones, pf[sg], l);
                st_c1(ws, lane < 16 ? SL_OFF + ((b * 32 + kb) * 32 + 16 * hg + i16) * 4 : OOB, asu(l[0]));
            }
        }
        STAMP(5);
        if (w == 6) check_flags(ws, want, lane);
        VMW(0);
        BAR();
        v4u vf[16];
        if (w < 4) {
            const int kbl = 2 * ((i16 + 4 * w) & 15) + (g & 1);
            const int so = ((g >> 1) * 32 + kbl) * 128 + dh * 4;
            unsigned mu;
            for (unsigned s = 0; s < SPIN_CAP; ++s) {
                CBAR();
                mu = ld_c1(ws, SM_OFF + so);
                if (__all(mu != SENT)) break;
            }
            const float mv = asf(mu);
            const float M = max16(rowmax(mv));
            const float wf = __builtin_amdgcn_exp2f(mv - M);
            const unsigned wb = pk2(wf, wf);
            const unsigned keep = ((i16 >> 3) == (g >> 1)) ? hm : 0u;
            v4u af[4];
            {
                unsigned d0 = dppu<DPP_BC + 0>(wb) & keep, d1 = dppu<DPP_BC + 1>(wb) & keep, d2 = dppu<DPP_BC + 2>(wb) & keep, d3 = dppu<DPP_BC + 3>(wb) & keep;
#pragma unroll
                for (int q = 0; q < 4; ++q) {
                    af[0][q] = hq == q ? d0 : 0u; af[1][q] = hq == q ? d1 : 0u; af[2][q] = hq == q ? d2 : 0u; af[3][q] = hq == q ? d3 : 0u;
                }
                PIN(af[0]); PIN(af[1]); PIN(af[2]); PIN(af[3]);
            }
            STAMP(6);
            v4u o[4];
            unsigned lu;
            const int oo = OP_OFF + ((((g >> 1) * 32 + dh) * 4 + dcq) * 32 + 8 * w + (g & 1)) * 256 + i16 * 16;
            for (unsigned s = 0; s < SPIN_CAP; ++s) {
                CBAR();
#pragma unroll
                for (int j = 0; j < 4; ++j) o[j] = ld_c(ws, oo + j * 512);
                lu = ld_c1(ws, SL_OFF + so);
                if (__all(umax(umax(umax(mx4(o[0]), mx4(o[1])), umax(mx4(o[2]), mx4(o[3]))), lu) != SENT)) break;
            }
            STAMP(7);
            f32x4 d = Z4;
#pragma unroll
            for (int j = 0; j < 4; ++j) d = mfma32(af[j], o[j], d);
            const float Ls = add16(rowsum(bflo(wb) * asf(lu)));
            d *= __builtin_amdgcn_rcpf(Ls);
            const unsigned h0 = pk2(d[0], d[1]), h1 = pk2(d[2], d[3]);
            const unsigned l0 = pk2(d[0] - bflo(h0), d[1] - bfhi(h0)), l1 = pk2(d[2] - bflo(h1), d[3] - bfhi(h1));
            LAS unsigned char* p = L + L_CL + ((g >> 1) * 8 + 2 * w) * 272 + (8 * i16 + 4 * (g & 1)) * 2;
            *(LAS v2u*)p = v2u{h0, h1};
            *(LAS v2u*)(p + 272) = v2u{l0, l1};
        } else if (w < 6) {
            const int key = ((tq >> 1) & 1) | ((g & 1) << 1);
            LAS unsigned char* tb = L + L_WV + (w - 4) * 16384 + (8 * g + tq) * 128 + tp * 8;
#pragma unroll
            for (int tau = 0; tau < 4; ++tau)
#pragma unroll
                for (int s = 0; s < 4; ++s) {
                    LAS unsigned char* p = tb + ((tau ^ key) * 32) + s * 4096;
                    vf[tau * 4 + s] = trd(p, p + 512);
                }
        }
        BAR();
        if (w >= 4 && w < 6) {
            v4u af[4];
#pragma unroll
            for (int s = 0; s < 4; ++s) af[s] = *(LAS v4u*)(L + L_CL + i16 * 272 + (32 * s + 8 * g) * 2);
            f32x4 d[4] = {Z4, Z4, Z4, Z4};
#pragma unroll
            for (int s = 0; s < 4; ++s)
#pragma unroll
                for (int tau = 0; tau < 4; ++tau) d[tau] = mfma32(af[s], vf[tau * 4 + s], d[tau]);
            const float e0 = rs16(sum4(d[0]), sum4(d[2])), e1 = rs16(sum4(d[1]), sum4(d[3]));
            const unsigned hi = pk2(e0, e1), lo = pk2(e0 - bflo(hi), e1 - bfhi(hi));
            const int co = CX_OFF + dh * 4096 + ((g >> 1) * 8 + dcq * 2) * 256 + (32 * (w - 4) + 16 * (g & 1) + i16) * 4;
            st_c1(ws, co, san(hi));
            st_c1(ws, co + 256, san(lo));
        }
        STAMP(8);
        DUMP();
        return;
    }

    const int ai = r - 16, h = 4 * xg + (ai >> 2), part = ai & 3;
    const bool isF = ai < 9;
    const int fu = 9 * xg + ai;
    const int e = ai - 9, kg = xg >> 1, et = (xg & 1) ? 5 + e : e;
    const bool isE = !isF && (e < ((xg & 1) ? 4 : 5));
    v2u want = {0u, 0u};
    if (w >= 4) want = ld_c2(ws, EPOCH_OFF);
    if (w == 7) {
        st_c(ws, QP_OFF + h * 4096 + ((g >> 1) * 8 + part * 2 + (g & 1)) * 256 + i16 * 16, SENT4);
        st_c(ws, lane < 32 ? QA_OFF + ((g * 32 + h) * 512 + 128 * part + 8 * i16) * 2 : OOB, SENT4);
        if (isE) st_c(ws, lane < 32 ? AO_OFF + ((rg >> 1) * 8 + kg * 2 + (rg & 1)) * 1152 + et * 128 + c8 * 16 : OOB, SENT4);
        if (isF) {
#pragma unroll
            for (int i = 0; i < 5; ++i) st_c(ws, i * 64 + lane < 288 ? FO_OFF + fu * 4608 + (i * 64 + lane) * 16 : OOB, SENT4);
        }
        VMW(0);
        want = next_epoch(want);
        if (lane == 0) st_c2(ws, FL_OFF + (xg * 32 + r) * 8, want);
        STAMP(1);
        BAR();
        BAR();
        f32x4 v = *(LAS f32x4*)(L + L_RA + lane * 16);
#pragma unroll
        for (int pp = 1; pp < 4; ++pp) v += *(LAS f32x4*)(L + L_RA + pp * 1024 + lane * 16);
        const unsigned h0 = pk2(v[0], v[1]), h1 = pk2(v[2], v[3]);
        const int qo = QP_OFF + h * 4096 + ((g >> 1) * 8 + part * 2) * 256 + (8 * i16 + 4 * (g & 1)) * 2;
        st_c2(ws, qo, v2u{san(h0), san(h1)});
        const unsigned l0 = pk2(v[0] - bflo(h0), v[1] - bfhi(h0)), l1 = pk2(v[2] - bflo(h1), v[3] - bfhi(h1));
        st_c2(ws, qo + 256, v2u{san(l0), san(l1)});
        STAMP(3);
    } else if (w < 4) {
        const int xo = (lane >> 5) * 1152 + (lane & 31) * 36;
        v4u x0 = __builtin_amdgcn_raw_buffer_load_b128(rx, xo, 0, 0), x1 = __builtin_amdgcn_raw_buffer_load_b128(rx, xo + 16, 0, 0);
        unsigned x2 = __builtin_amdgcn_raw_buffer_load_b32(rx, xo + 32, 0, 0);
        const int xb = (i16 >> 3) * 576 + 144 * part;
        v4u xr = __builtin_amdgcn_raw_buffer_load_b128(rx, (xb + 32 * w + 8 * g) * 2, 0, 0);
        unsigned xe = __builtin_amdgcn_raw_buffer_load_b16(rx, (xb + 128 + 4 * w + g) * 2, 0, 0);
        const rsrc_t rq = mk(a.wq, 576 * 4096 * 2);
        v4u wq[9];
        const int qo = (144 * part + 32 * w + 8 * g) * 8192 + h * 256 + i16 * 16;
#pragma unroll
        for (int j = 0; j < 8; ++j) wq[j] = ld_nt(rq, qo + j * 8192);
        wq[8] = ld_nt(rq, (144 * part + 128 + 4 * w + g) * 8192 + h * 256 + i16 * 16);
        __builtin_amdgcn_sched_barrier(0);
        BAR();
        float xv[18];
#pragma unroll
        for (int q = 0; q < 4; ++q) { xv[2 * q] = bflo(x0[q]); xv[2 * q + 1] = bfhi(x0[q]); xv[8 + 2 * q] = bflo(x1[q]); xv[9 + 2 * q] = bfhi(x1[q]); }
        xv[16] = bflo(x2); xv[17] = bfhi(x2);
        float s = 0.f;
#pragma unroll
        for (int q = 0; q < 18; ++q) s += xv[q];
        float mean = add16(rowsum(s)) * (1.0f / 576.0f);
        float vs = 0.f;
#pragma unroll
        for (int q = 0; q < 18; ++q) { float d = xv[q] - mean; vs += d * d; }
        float rs = __builtin_amdgcn_rsqf(add16(rowsum(vs)) * (1.0f / 576.0f) + 1e-5f) * (0.08838834764831845f * 1.4426950408889634f);
        v4u mk4, af[9], ones;
#pragma unroll
        for (int q = 0; q < 4; ++q) { mk4[q] = hq == q ? hm : 0u; ones[q] = mk4[q] & 0x3F803F80u; }
#pragma unroll
        for (int j = 0; j < 9; ++j) {
            const unsigned src = j < 8 ? xr[j >> 1] : xe;
            const unsigned dup = __builtin_amdgcn_perm(src, src, (j & 1) ? 0x03020302u : 0x01000100u);
#pragma unroll
            for (int q = 0; q < 4; ++q) af[j][q] = dup & mk4[q];
            PIN(af[j]);
        }
        PIN(ones); PIN(mean); PIN(rs);
        STAMP(1);
        f32x4 acc = Z4, cs = Z4;
#pragma unroll
        for (int j = 0; j < 9; ++j) { acc = mfma32(af[j], wq[j], acc); cs = mfma32(ones, wq[j], cs); }
        *(LAS f32x4*)(L + L_RA + w * 1024 + lane * 16) = (acc - cs * mean) * rs;
        STAMP(2);
        BAR();
    } else if (w < 6) {
        BAR();
        const int wb = w - 4;
        {
            const v4u sk = mks(a.wuk, 512 * 4096 * 2);
            const int so = (128 * part + 64 * wb) * 8192 + h * 256;
#pragma unroll
            for (int t = 0; t < 16; ++t) dma(sk, g * 8192 + ((i16 ^ t) * 16), so + t * 4 * 8192, L_UK + wb * 16384 + t * 1024);
        }
        want = next_epoch(want);
        const int fo = FL_OFF + (xg * 32 + 16 + (ai & ~3) + (lane & 3)) * 8;
        BAR();
        v2u fl = ld_c2(ws, fo);
        VMW(0);
        STAMP(4);
        v4u uk[16];
#pragma unroll
        for (int tau = 0; tau < 4; ++tau)
#pragma unroll
            for (int s = 0; s < 4; ++s) uk[tau * 4 + s] = *(LAS v4u*)(L + L_UK + wb * 16384 + i16 * 1024 + tau * 256 + (((4 * s + g) ^ i16) * 16));
        for (unsigned s = 0; s < SPIN_CAP; ++s) {
            if (__all(((fl.x ^ want.x) | (fl.y ^ want.y)) == 0u)) break;
            CBAR();
            fl = ld_c2(ws, fo);
        }
        v4u qf[4];
        const int po = QP_OFF + h * 4096 + i16 * 256 + g * 16;
        int qa = QA_OFF + (((g >> 1) * 32 + h) * 512 + 128 * part + 64 * wb + 4 * i16 + 2 * (g & 1)) * 2;
        PIN(qa);
        for (unsigned s = 0; s < SPIN_CAP; ++s) {
            CBAR();
#pragma unroll
            for (int j = 0; j < 4; ++j) qf[j] = ld_c(ws, po + j * 64);
            if (__all(umax(umax(mx4(qf[0]), mx4(qf[1])), umax(mx4(qf[2]), mx4(qf[3]))) != SENT)) break;
        }
        STAMP(5);
        f32x4 d[4] = {Z4, Z4, Z4, Z4};
#pragma unroll
        for (int s = 0; s < 4; ++s)
#pragma unroll
            for (int tau = 0; tau < 4; ++tau) d[tau] = mfma32(qf[s], uk[tau * 4 + s], d[tau]);
        const float e0 = rs16(sum4(d[0]), sum4(d[2])), e1 = rs16(sum4(d[1]), sum4(d[3]));
        st_c1(ws, qa, san(pk2(e0, e1)));
        STAMP(6);
    } else {
        want = next_epoch(want);
        BAR();
        BAR();
    }
    BAR();

    if (isF) {
        const v4u sg_ = mks(a.wg, 576 * 4608 * 2), su_ = mks(a.wu, 576 * 4608 * 2), sd_ = mks(a.wd, 4608 * 576 * 2);
        {
            const int key = ((rg >> 1) & 1) | ((w & 1) << 1);
            const int vd = rg * 1152 + ((c8 ^ (key << 1)) * 16);
#pragma unroll
            for (int bl = 0; bl < 9; ++bl) dma(sd_, vd, (64 * fu + 8 * w) * 1152 + bl * 128, L_DN + bl * 8192 + w * 1024);
        }
        const int keyg = (((w & 3) >> 1) & 1) | (((rg >> 2) & 1) << 1);
        const int vg = rg * 9216 + ((c8 ^ (keyg << 1)) * 16);
        const v4u sgu = (w >> 2) ? su_ : sg_;
        const int sgo = 8 * (w & 3) * 9216 + fu * 128;
#pragma unroll
        for (int s = 0; s < 10; ++s) dma(sgu, vg, sgo + s * 32 * 9216, L_GU1 + s * 8192 + w * 1024);
        VMW(10);
        BAR();
        v4u df[10];
        {
            const int key = ((tq >> 1) & 1) | ((g & 1) << 1);
            LAS unsigned char* tb = L + L_DN + (w >> 2) * 8192 + (8 * g + tq) * 128 + tp * 8 + (((w & 3) ^ key) * 32);
#pragma unroll
            for (int i = 0; i < 5; ++i)
#pragma unroll
                for (int s = 0; s < 2; ++s) {
                    LAS unsigned char* p = tb + (i < 4 ? i : (w < 4 ? 4 : 0)) * 16384 + s * 4096;
                    df[i * 2 + s] = trd(p, p + 512);
                }
        }
        BAR();
#pragma unroll
        for (int s = 10; s < 18; ++s) dma(sgu, vg, sgo + s * 32 * 9216, L_GU2 + (s - 10) * 8192 + w * 1024);
        VMW(8);
        BAR();
        v4u gf[18], uf[18];
        const int keyp = (tq & 1) | ((g & 1) << 1);
        const int tro = (16 * (tq & 1) + 4 * g + (tq >> 1)) * 128 + tp * 8 + (((w & 3) ^ keyp) * 32);
        if (w < 4) {
#pragma unroll
            for (int s = 0; s < 10; ++s) {
                LAS unsigned char* p = L + L_GU1 + s * 8192 + tro;
                gf[s] = trd(p, p + 256);
                uf[s] = trd(p + 4096, p + 4096 + 256);
            }
        }
        if (w == 6) check_flags(ws, want, lane);
        VMW(0);
        BAR();
        STAMP(7);
        const v4u ones = {0x3F803F80u, 0x3F803F80u, 0x3F803F80u, 0x3F803F80u};
        float csg = 0.f, csu = 0.f;
        if (w < 4) {
#pragma unroll
            for (int s = 10; s < 18; ++s) {
                LAS unsigned char* p = L + L_GU2 + (s - 10) * 8192 + tro;
                gf[s] = trd(p, p + 256);
                uf[s] = trd(p + 4096, p + 4096 + 256);
            }
            f32x4 cg = Z4, cu = Z4;
#pragma unroll
            for (int s = 0; s < 18; ++s) { cg = mfma32(ones, gf[s], cg); cu = mfma32(ones, uf[s], cu); }
            csg = cg[0]; csu = cu[0];
            PIN(csg); PIN(csu);
        } else {
            const int wq_ = w - 4;
            int go[5], lo_[5];
#pragma unroll
            for (int i = 0; i < 5; ++i) {
                const int id = i * 64 + lane;
                go[i] = id < 288 ? AO_OFF + wq_ * 4608 + id * 16 : OOB;
                lo_[i] = id < 288 ? L_AO + wq_ * 4 * AOP + id * 16 + (id / 72) * 16 : L_AO + 16 * AOP + lane * 16;
                PIN(lo_[i]);
            }
            v4u av[5];
            for (unsigned s = 0; s < SPIN_CAP; ++s) {
                CBAR();
#pragma unroll
                for (int i = 0; i < 5; ++i) av[i] = ld_c(ws, go[i]);
                if (__all(umax(umax(umax(mx4(av[0]), mx4(av[1])), umax(mx4(av[2]), mx4(av[3]))), mx4(av[4])) != SENT)) break;
            }
            STAMP(8);
#pragma unroll
            for (int i = 0; i < 5; ++i) *(LAS v4u*)(L + lo_[i]) = av[i];
        }
        BAR();
        LAS unsigned char* ab = L + L_AO + i16 * AOP + g * 16;
        f32x4 dg = Z4, du = Z4;
        if (w < 4) {
#pragma unroll
            for (int s = 0; s < 18; ++s) {
                v4u af = *(LAS v4u*)(ab + s * 64);
                dg = mfma32(af, gf[s], dg);
                du = mfma32(af, uf[s], du);
            }
        } else {
            const int wq_ = w - 4;
#pragma unroll
            for (int i = 0; i < 5; ++i) {
                if (i < 4 || wq_ < 2) {
                    v4u af = *(LAS v4u*)(ab + (wq_ + 4 * i) * 64);
                    dg = mfma32(af, af, dg);
                    du = mfma32(af, ones, du);
                }
            }
            float s2 = ((i16 >> 3) == (g >> 1)) ? sum4(dg) : 0.f;
            s2 = rowsum(s2);
            const float st = rs16(sum4(du), s2);
            if (i16 == 0) *(LAS float*)(L + L_FS + (g * 4 + wq_) * 4) = st;
        }
        BAR();
        if (w < 4) {
            const f32x4 s1 = *(LAS f32x4*)(L + L_FS + (g >> 1) * 32), s2 = *(LAS f32x4*)(L + L_FS + (g >> 1) * 32 + 16);
            const float gs = add16(sum4(dg)), us = add16(sum4(du));
            const float mean = sum4(s1) * (1.0f / 576.0f);
            const float var = fmaxf(sum4(s2) * (1.0f / 576.0f) - mean * mean, 0.f);
            const float rstd = __builtin_amdgcn_rsqf(var + 1e-5f);
            const float gg = (gs - mean * csg) * rstd, uu = (us - mean * csu) * rstd;
            const float act = gg * uu * __builtin_amdgcn_rcpf(1.0f + __builtin_amdgcn_exp2f(gg * -1.4426950408889634f));
            if ((g & 1) == 0) *(LAS unsigned short*)(L + L_ACT + (g >> 1) * 128 + (16 * w + i16) * 2) = (unsigned short)pk2(act, act);
        }
        BAR();
        {
            const v4u b0 = *(LAS v4u*)(L + L_ACT + (i16 & 1) * 128 + g * 16), b1 = *(LAS v4u*)(L + L_ACT + (i16 & 1) * 128 + 64 + g * 16);
            const int fo = i16 < 2 ? FO_OFF + fu * 4608 + w * 128 + i16 * 64 + g * 16 : OOB;
#pragma unroll
            for (int i = 0; i < 5; ++i) {
                if (i < 4 || w < 4) {
                    f32x4 d = mfma32(df[2 * i + 1], b1, mfma32(df[2 * i], b0, Z4));
                    st_c(ws, fo + i * 1024, __builtin_bit_cast(v4u, d));
                }
            }
        }
        STAMP(9);
        DUMP();
        return;
    }
    if (!isE) {
        if (blockIdx.x == 255 && w == 6) {
            check_flags(ws, want, lane);
            if (lane == 0) st_c2(ws, EPOCH_OFF, want);
        }
        DUMP();
        return;
    }
    {
        const int hh = 8 * kg + w;
        {
            const v4u so_ = mks(a.wo, 4096 * 576 * 2);
            const int k1 = ((rg >> 2) & 1) << 1;
            const int v0 = rg * 1152 + ((c8 ^ (k1 << 1)) * 16), v1 = rg * 1152 + ((c8 ^ ((k1 | 1) << 1)) * 16);
#pragma unroll
            for (int n = 0; n < 16; ++n) dma(so_, ((n >> 1) & 1) ? v1 : v0, (hh * 128 + 8 * n) * 1152 + et * 128, L_WO + w * 16384 + n * 1024);
        }
        const int xc = (g >> 1) * 576 + 64 * et + 32 * (g & 1) + i16;
        unsigned xa = __builtin_amdgcn_raw_buffer_load_b16(rx, kg == 0 ? xc * 2 : OOB, 0, 0);
        unsigned xb2 = __builtin_amdgcn_raw_buffer_load_b16(rx, kg == 0 ? (xc + 16) * 2 : OOB, 0, 0);
        if (w == 6) check_flags(ws, want, lane);
        VMW(0);
        v4u wf[16];
        {
            const int keyp = (tq & 1) | ((g & 1) << 1);
            LAS unsigned char* tb = L + L_WO + w * 16384 + (16 * (tq & 1) + 4 * g + (tq >> 1)) * 128 + tp * 8;
#pragma unroll
            for (int tau = 0; tau < 4; ++tau)
#pragma unroll
                for (int s = 0; s < 4; ++s) {
                    LAS unsigned char* p = tb + ((tau ^ keyp) * 32) + s * 4096;
                    wf[tau * 4 + s] = trd(p, p + 256);
                }
        }
        BAR();
        STAMP(7);
        v4u cf[4];
        const int co = CX_OFF + hh * 4096 + i16 * 256 + g * 16;
        for (unsigned s = 0; s < SPIN_CAP; ++s) {
            CBAR();
#pragma unroll
            for (int j = 0; j < 4; ++j) cf[j] = ld_c(ws, co + j * 64);
            if (__all(umax(umax(mx4(cf[0]), mx4(cf[1])), umax(mx4(cf[2]), mx4(cf[3]))) != SENT)) break;
        }
        STAMP(8);
        {
            f32x4 d[4] = {Z4, Z4, Z4, Z4};
#pragma unroll
            for (int s = 0; s < 4; ++s)
#pragma unroll
                for (int tau = 0; tau < 4; ++tau) d[tau] = mfma32(cf[s], wf[tau * 4 + s], d[tau]);
            const float e0 = rs16(sum4(d[0]), sum4(d[2])), e1 = rs16(sum4(d[1]), sum4(d[3]));
            *(LAS f32x2*)(L + L_RE + w * 512 + lane * 8) = f32x2{e0, e1};
        }
        BAR();
        const int ru = 9 * kg + et;
        if (w == 0) {
            f32x2 v = {bflo(xa), bflo(xb2)};
#pragma unroll
            for (int pp = 0; pp < 8; ++pp) v += *(LAS f32x2*)(L + L_RE + pp * 512 + lane * 8);
            const unsigned hi = pk2(v[0], v[1]), lo = pk2(v[0] - bflo(hi), v[1] - bfhi(hi));
            const int ao = AO_OFF + ((g >> 1) * 8 + kg * 2) * 1152 + et * 128 + (16 * (g & 1) + i16) * 4;
            st_c1(ws, ao, san(hi));
            st_c1(ws, ao + 1152, san(lo));
            STAMP(9);
        } else if (w == 1) {
            v4u hv;
            const int ho = AO_OFF + ((c8 >> 2) * 8 + rg) * 1152 + (ru >> 1) * 64 + (c8 & 3) * 16;
            for (unsigned s = 0; s < SPIN_CAP; ++s) {
                CBAR();
                hv = ld_c(ws, ho);
                if (__all(mx4(hv) != SENT)) break;
            }
            f32x4 acc;
#pragma unroll
            for (int j = 0; j < 4; ++j) acc[j] = (ru & 1) ? bfhi(hv[j]) : bflo(hv[j]);
            PIN(acc);
            v4u fv[9];
            const int fo = FO_OFF + rg * 4608 + ru * 128 + c8 * 16;
            for (unsigned s = 0; s < SPIN_CAP; ++s) {
                CBAR();
#pragma unroll
                for (int j = 0; j < 9; ++j) fv[j] = ld_c(ws, fo + j * 8 * 4608);
                unsigned m = mx4(fv[0]);
#pragma unroll
                for (int j = 1; j < 9; ++j) m = umax(m, mx4(fv[j]));
                if (__all(m != SENT)) break;
            }
            STAMP(10);
#pragma unroll
            for (int j = 0; j < 9; ++j) acc += __builtin_bit_cast(f32x4, fv[j]);
            float u0 = rs32(acc[0], acc[2]), u1 = rs32(acc[1], acc[3]);
            u0 = add16(u0); u1 = add16(u1);
            u0 += dpp<DPP_ROR8>(u0); u1 += dpp<DPP_ROR8>(u1);
            if ((lane & 31) < 8)
                *(unsigned*)((unsigned char*)a.out + ((c8 >> 2) * 576 + 16 * ru + 4 * (c8 & 3) + 2 * (lane >> 5)) * 2) = pk2(u0, u1);
            STAMP(11);
        }
        DUMP();
    }
}

}

extern "C" void kernel_launch(void* const* d_in, const int* in_sizes, int n_in, void* d_out, int out_size, void* d_ws,
                              size_t ws_size, hipStream_t stream) {
    Args a;
    a.x = d_in[0]; a.ckv = d_in[1]; a.wq = d_in[2]; a.wuk = d_in[3]; a.wuv = d_in[4]; a.wo = d_in[5];
    a.wg = d_in[7]; a.wu = d_in[8]; a.wd = d_in[9];
    a.out = d_out; a.ws = (unsigned char*)d_ws;
    (void)hipFuncSetAttribute((const void*)fused, hipFuncAttributeMaxDynamicSharedMemorySize, LDS_BYTES);
    hipLaunchKernelGGL(fused, dim3(256), dim3(NT), LDS_BYTES, stream, a);
}
```

```cpp
#include <hip/hip_runtime.h>
#include <hip/hip_bf16.h>

#define LAS __attribute__((address_space(3)))
typedef unsigned v4u __attribute__((ext_vector_type(4)));
typedef unsigned v2u __attribute__((ext_vector_type(2)));
typedef float f32x4 __attribute__((ext_vector_type(4)));
typedef float f32x2 __attribute__((ext_vector_type(2)));
typedef short s16x4 __attribute__((ext_vector_type(4)));
typedef __bf16 bf16x8 __attribute__((ext_vector_type(8)));
typedef __bf16 bf16x2 __attribute__((ext_vector_type(2)));
typedef __amdgpu_buffer_rsrc_t rsrc_t;
#define DI __device__ __forceinline__

#define STAMPS 0

#pragma clang diagnostic ignored "-Winline-asm"
#define KVER 8
namespace {

constexpr int NT = 512, NW = 8;
constexpr unsigned SENT = 0xFFFFFFFFu;
constexpr int AUX_NT = 2, AUX_SC1 = 16;
constexpr unsigned SPIN_CAP = 1u << 18;
constexpr int OOB = 0x7ffff000;

constexpr int EPOCH_OFF = 0;
constexpr int FL_OFF = 1024;
constexpr int FLQ_OFF = 4096;
constexpr int QP_OFF = 8192;
constexpr int QA_OFF = QP_OFF + 262144;
constexpr int SM_OFF = QA_OFF + 65536;
constexpr int SL_OFF = SM_OFF + 8192;
constexpr int CX_OFF = SL_OFF + 8192;
constexpr int AO_OFF = CX_OFF + 65536;
constexpr int FO_OFF = AO_OFF + 9216;
constexpr int OP_OFF = 1 << 20;
constexpr int WS_BYTES = OP_OFF + (2 << 20);
constexpr int DBG_OFF = 8 << 20;

constexpr int L_CK = 0;
constexpr int L_Q = 131072;
constexpr int SPITCH = 528;
constexpr int L_S = L_Q + 16384;
constexpr int L_MX = L_S + 16 * SPITCH;
constexpr int L_WV = 0;
constexpr int L_CL = L_Q;
constexpr int L_UK = 0;
constexpr int L_DN = 0;
constexpr int L_GU1 = 73728;
constexpr int L_GU2 = 0;
constexpr int L_AO = 73728;
constexpr int AOP = 1168;
constexpr int L_FS = 155648;
constexpr int L_ACT = L_FS + 64;
constexpr int L_WO = 0;
constexpr int L_RE = 131072;
constexpr int L_DBG = 156672;
constexpr int L_JUNK = L_DBG + 1024;
constexpr int LDS_BYTES = L_JUNK + 1024;
static_assert(L_MX + 256 <= L_DBG && L_ACT + 256 <= L_DBG, "lds map");
__host__ __device__ constexpr int ckey(int row) { return ((((row >> 3) ^ (row >> 2)) & 1) << 3) | (((row >> 1) & 1) << 2) | ((row & 1) << 1) | ((row >> 2) & 1); }
constexpr unsigned long long ckey_tab() { unsigned long long t = 0; for (int i = 0; i < 16; ++i) t |= (unsigned long long)ckey(i) << (4 * i); return t; }

DI v4u ld_nt(rsrc_t r, int off) { return __builtin_amdgcn_raw_buffer_load_b128(r, off, 0, AUX_NT); }
DI v4u ld_c(rsrc_t r, int off) { return __builtin_amdgcn_raw_buffer_load_b128(r, off, 0, AUX_SC1); }
DI v2u ld_c2(rsrc_t r, int off) { return __builtin_amdgcn_raw_buffer_load_b64(r, off, 0, AUX_SC1); }
DI unsigned ld_c1(rsrc_t r, int off) { return __builtin_amdgcn_raw_buffer_load_b32(r, off, 0, AUX_SC1); }
DI void st_c(rsrc_t r, int off, v4u v) { __builtin_amdgcn_raw_buffer_store_b128(v, r, off, 0, AUX_SC1); }
DI void st_c2(rsrc_t r, int off, v2u v) { __builtin_amdgcn_raw_buffer_store_b64(v, r, off, 0, AUX_SC1); }
DI void st_c1(rsrc_t r, int off, unsigned v) { __builtin_amdgcn_raw_buffer_store_b32(v, r, off, 0, AUX_SC1); }
DI unsigned san(unsigned x) { return x < 0xFFFFFFFEu ? x : 0xFFFFFFFEu; }
DI rsrc_t mk(const void* p, unsigned bytes) { return __builtin_amdgcn_make_buffer_rsrc((void*)p, 0, bytes, 0x00020000); }
DI v4u mks(const void* p, unsigned bytes) {
    const unsigned long long a = (unsigned long long)p;
    return v4u{(unsigned)a, (unsigned)(a >> 32) & 0xFFFFu, bytes, 0x00020000u};
}
DI void dma(v4u srd, int voff, int soff, int lds) {
    asm volatile("s_mov_b32 m0, %0\n\ts_nop 0\n\tbuffer_load_dwordx4 %1, %2, %3 offen nt lds" ::"s"(lds), "v"(voff), "s"(srd), "s"(soff) : "m0", "memory");
}
DI void dmap(v4u srd, int voff, int soff, int lds) {
    asm volatile("s_mov_b32 m0, %0\n\ts_nop 0\n\tbuffer_load_dwordx4 %1, %2, %3 offen lds" ::"s"(lds), "v"(voff), "s"(srd), "s"(soff) : "m0", "memory");
}
DI float asf(unsigned u) { return __builtin_bit_cast(float, u); }
DI unsigned asu(float f) { return __builtin_bit_cast(unsigned, f); }
DI float bflo(unsigned u) { return asf(u << 16); }
DI float bfhi(unsigned u) { return asf(u & 0xFFFF0000u); }
DI unsigned pk2(float a, float b) { bf16x2 t = {(__bf16)a, (__bf16)b}; return __builtin_bit_cast(unsigned, t); }
DI f32x4 mfma32(v4u a, v4u b, f32x4 c) {
    return __builtin_amdgcn_mfma_f32_16x16x32_bf16(__builtin_bit_cast(bf16x8, a), __builtin_bit_cast(bf16x8, b), c, 0, 0, 0);
}
template <int CTRL> DI float dpp(float v) { return asf((unsigned)__builtin_amdgcn_update_dpp(0, (int)asu(v), CTRL, 0xf, 0xf, true)); }
template <int CTRL> DI unsigned dppu(unsigned v) { return (unsigned)__builtin_amdgcn_update_dpp(0, (int)v, CTRL, 0xf, 0xf, true); }
constexpr int DPP_X1 = 0xB1, DPP_X2 = 0x4E, DPP_HMIR = 0x141, DPP_ROR8 = 0x128, DPP_ROR4 = 0x124, DPP_ROR2 = 0x122, DPP_ROR1 = 0x121;
constexpr int DPP_BC = 0x150;
DI float rs16(float a, float b) { auto r = __builtin_amdgcn_permlane16_swap(asu(a), asu(b), false, false); unsigned x = r[0], y = r[1]; return asf(x) + asf(y); }
DI float rs32(float a, float b) { auto r = __builtin_amdgcn_permlane32_swap(asu(a), asu(b), false, false); unsigned x = r[0], y = r[1]; return asf(x) + asf(y); }
DI float add16(float v) { return rs16(v, v); }
DI float add32(float v) { return rs32(v, v); }
DI float max16(float v) { auto r = __builtin_amdgcn_permlane16_swap(asu(v), asu(v), false, false); unsigned a = r[0], b = r[1]; return fmaxf(asf(a), asf(b)); }
DI float max32(float v) { auto r = __builtin_amdgcn_permlane32_swap(asu(v), asu(v), false, false); unsigned a = r[0], b = r[1]; return fmaxf(asf(a), asf(b)); }
DI float rowsum(float v) { v += dpp<DPP_ROR8>(v); v += dpp<DPP_ROR4>(v); v += dpp<DPP_ROR2>(v); v += dpp<DPP_ROR1>(v); return v; }
DI float rowmax(float v) { v = fmaxf(v, dpp<DPP_ROR8>(v)); v = fmaxf(v, dpp<DPP_ROR4>(v)); v = fmaxf(v, dpp<DPP_ROR2>(v)); v = fmaxf(v, dpp<DPP_ROR1>(v)); return v; }
DI float sum4(f32x4 d) { return (d[0] + d[2]) + (d[1] + d[3]); }
DI unsigned umax(unsigned a, unsigned b) { return a > b ? a : b; }
DI unsigned mx4(v4u v) { return umax(umax(v.x, v.y), umax(v.z, v.w)); }
DI v4u trd(LAS unsigned char* p0, LAS unsigned char* p1) {
    v2u a = __builtin_bit_cast(v2u, __builtin_amdgcn_ds_read_tr16_b64_v4i16((LAS s16x4*)p0));
    v2u b = __builtin_bit_cast(v2u, __builtin_amdgcn_ds_read_tr16_b64_v4i16((LAS s16x4*)p1));
    return v4u{a.x, a.y, b.x, b.y};
}
#define CBAR() asm volatile("" ::: "memory")
#define VMW(n) asm volatile("s_waitcnt vmcnt(" #n ")" ::: "memory")
#define BAR() asm volatile("s_waitcnt lgkmcnt(0)\n\ts_barrier" ::: "memory")
#define PIN(v) asm volatile("" : "+v"(v))
#define SB() __builtin_amdgcn_sched_barrier(0)
#define OPQ(p) asm volatile("" : "+s"(p))
constexpr f32x4 Z4 = {0.f, 0.f, 0.f, 0.f};

DI void check_flags(rsrc_t ws, v2u want, int lane) {
    for (unsigned s = 0; s < SPIN_CAP; ++s) {
        CBAR();
        v4u a = ld_c(ws, FL_OFF + lane * 16), b = ld_c(ws, FL_OFF + 1024 + lane * 16);
        unsigned d = (a.x ^ want.x) | (a.y ^ want.y) | (a.z ^ want.x) | (a.w ^ want.y) | (b.x ^ want.x) | (b.y ^ want.y) | (b.z ^ want.x) | (b.w ^ want.y);
        if (__all(d == 0u)) break;
    }
    CBAR();
}
DI v2u load_epoch(const unsigned char* ws) {
    const unsigned long long e = *(const __attribute__((address_space(4))) unsigned long long*)(ws + EPOCH_OFF) + 1ull;
    return v2u{(unsigned)e, (unsigned)(e >> 32)};
}

#define STAMP(i) do {} while (0)
#define DUMP() do {} while (0)
#define STAMP_INIT() do {} while (0)

struct Args {
    const void *x, *ckv, *wq, *wuk, *wuv, *wo, *wg, *wu, *wd;
    void* out;
    unsigned char* ws;
};
#define G1_DELAY 8
#define B_DELAY 8
#define E_DELAY 4
#define F_DELAY 0
#define R_DELAY 20
#define CODE_KB 19

DI void dma_loop(v4u srd, int n, int vhi, int i16, unsigned long long tab, int soff, int sstep, int lds, int lstep = 1024) {
    soff = __builtin_amdgcn_readfirstlane(soff); lds = __builtin_amdgcn_readfirstlane(lds);
    asm volatile("s_nop 4" : "+s"(soff), "+s"(lds));
#pragma unroll 1
    for (int i = 0; i < n; ++i) {
        dma(srd, vhi + ((i16 ^ (int)((unsigned)tab & 15u)) << 4), soff, lds);
        tab >>= 4; soff += sstep; lds += lstep;
    }
}
DI void wait_flags16(rsrc_t ws, int off, v4u fl, v2u want) {
    for (unsigned s = 0; s < SPIN_CAP; ++s) {
        if (__all(((fl.x ^ want.x) | (fl.y ^ want.y) | (fl.z ^ want.x) | (fl.w ^ want.y)) == 0u)) break;
        CBAR();
        fl = ld_c(ws, off);
    }
    CBAR();
}

__global__ void __launch_bounds__(NT, 2) fused(Args a) {
    extern __shared__ __attribute__((aligned(16))) unsigned char smem[];
    LAS unsigned char* L = (LAS unsigned char*)smem;
    const int tid = threadIdx.x, lane = tid & 63, w = __builtin_amdgcn_readfirstlane(tid >> 6);
    const int xg = blockIdx.x & 7, r = blockIdx.x >> 3;
    const int c8 = lane & 7, rg = lane >> 3, i16 = lane & 15, g = lane >> 4;
    const int tq = i16 >> 2, tp = i16 & 3;
    const v4u SENT4 = {SENT, SENT, SENT, SENT};
    const int dh = 4 * xg + ((r & 15) >> 2), dcq = r & 3;
    const unsigned hm = (i16 & 1) ? 0xFFFF0000u : 0x0000FFFFu;
    const int hq = (i16 >> 1) & 3;
    const v4u ONES = {0x3F803F80u, 0x3F803F80u, 0x3F803F80u, 0x3F803F80u};
    if (w == 7 && r < CODE_KB) {
        unsigned long long pc;
        asm volatile("s_getpc_b64 %0" : "=s"(pc));
        pc &= ~0xFFull;
        const v4u sc = v4u{(unsigned)pc, (unsigned)(pc >> 32) & 0xFFFFu, (unsigned)(CODE_KB * 1024), 0x00020000u};
        dmap(sc, lane * 16, r * 1024, L_JUNK);
    }
    STAMP_INIT();
    STAMP(0);
    v2u want;

    const int myfl = FL_OFF + ((r >> 4) * 128 + xg * 16 + (r & 15)) * 8;
    if (r < 16) {
        const int b = xg >> 2, kb = 8 * (xg & 3) + (r >> 1), hg = r & 1;
        unsigned char* wsp = a.ws;
        const int fo = FLQ_OFF + (w * 16 + i16) * 16;
        v4u fl;
#define QGATHER() \
        int ql0 = L_Q + w * 1024 + ((lane ^ ckey(w)) * 16), ql1 = L_Q + (8 + w) * 1024 + ((lane ^ ckey(8 + w)) * 16); \
        int qoff = QA_OFF + (b * 32 + 16 * hg) * 1024 + tid * 16; \
        PIN(ql0); PIN(ql1); PIN(qoff); \
        VMW(0); \
        OPQ(want); \
        STAMP(1); \
        wait_flags16(ws, fo, fl, want); \
        BAR(); \
        STAMP(2); \
        v4u q0, q1; \
        for (unsigned s = 0; s < SPIN_CAP; ++s) { \
            CBAR(); \
            q0 = ld_c(ws, qoff); q1 = ld_c(ws, qoff + 8192); \
            if (__all(umax(mx4(q0), mx4(q1)) != SENT)) break; \
        } \
        STAMP(3); \
        *(LAS v4u*)(L + ql0) = q0; \
        *(LAS v4u*)(L + ql1) = q1; \
        BAR();
        if (w < 4) {
            const int key = ckey(i16);
            v4u fr[32];
            const unsigned long long tab = (w & 1) ? (ckey_tab() >> 32) : ckey_tab();
            const int cso = (b * 4096 + 128 * kb + 8 * w) * 1024;
            int vhi = g * 256;
            PIN(vhi);
            SB();
            const void* ckvp = a.ckv;
            OPQ(wsp); OPQ(ckvp);
            const rsrc_t ws = mk(wsp, WS_BYTES);
            const v4u sck = mks(ckvp, 2 * 4096 * 512 * 2);
#define CKV(sg) dma_loop(sck, 8, vhi, i16, tab, cso + sg * 32768, 1024, L_CK + sg * 32768 + w * 8192)
#define SFR() do { LAS unsigned char* sb = L + L_CK + (32 * w + i16) * 1024; \
                _Pragma("unroll") for (int s = 0; s < 16; ++s) { \
                    LAS unsigned char* p = sb + ((((4 * s + g) & 15) ^ key) * 16) + (s >> 2) * 256; \
                    fr[s] = *(LAS v4u*)p; fr[16 + s] = *(LAS v4u*)(p + 16384); } } while (0)
#define LSTEP0() do { VMW(16); BAR(); CKV(3); } while (0)
#define LSTEP1() do { VMW(16); BAR(); } while (0)
#define LSTEP2() do { VMW(8); BAR(); } while (0)
#define LSTEP3() do { fl = ld_c(ws, fo); VMW(1); BAR(); } while (0)
            __builtin_amdgcn_s_sleep(G1_DELAY);
            CKV(0); CKV(1); CKV(2);
            want = load_epoch(wsp);
            LSTEP0();
            if (w > 0) { LSTEP1(); if (w > 1) { LSTEP2(); if (w > 2) LSTEP3(); } }
            SFR();
            if (w < 1) LSTEP1();
            if (w < 2) LSTEP2();
            if (w < 3) LSTEP3();
            QGATHER();
            f32x4 s0 = Z4, s1 = Z4;
            LAS unsigned char* qb = L + L_Q + i16 * 1024;
            v4u qf[16];
#pragma unroll
            for (int s = 0; s < 16; ++s) qf[s] = *(LAS v4u*)(qb + ((((4 * s + g) & 15) ^ key) * 16) + (s >> 2) * 256);
            SB();
#pragma unroll
            for (int s = 0; s < 16; ++s) {
                s0 = mfma32(fr[s], qf[s], s0);
                s1 = mfma32(fr[16 + s], qf[s], s1);
            }
            LAS unsigned char* sp = L + L_S + i16 * SPITCH + (32 * w + 4 * g) * 4;
            *(LAS f32x4*)sp = s0;
            *(LAS f32x4*)(sp + 64) = s1;
            float mx = fmaxf(fmaxf(fmaxf(s0[0], s0[1]), fmaxf(s0[2], s0[3])), fmaxf(fmaxf(s1[0], s1[1]), fmaxf(s1[2], s1[3])));
            mx = max32(max16(mx));
            if (lane < 16) *(LAS float*)(L + L_MX + (i16 * 4 + w) * 4) = mx;
            STAMP(4);
            BAR();
            if (w == 0) {
                const f32x4 m4 = *(LAS f32x4*)(L + L_MX + i16 * 16);
                const float m = fmaxf(fmaxf(m4[0], m4[1]), fmaxf(m4[2], m4[3]));
                st_c1(ws, lane < 16 ? SM_OFF + ((b * 32 + kb) * 32 + 16 * hg + i16) * 4 : OOB, asu(m));
            }
            const int kbl = (2 * ((i16 + 4 * w) & 15) + (g & 1) - 4 * dh - dcq) & 31;
            int so = ((g >> 1) * 32 + kbl) * 128 + dh * 4;
            int oo = OP_OFF + (((g >> 1) * 32 + dh) * 4 + dcq) * 8192 + (8 * w + (g & 1)) * 256 + i16 * 16;
            int clo = L_CL + ((g >> 1) * 8 + 2 * w) * 272 + (8 * i16 + 4 * (g & 1)) * 2;
            PIN(clo); PIN(so); PIN(oo);
            __builtin_amdgcn_s_sleep(4);
            v4u fl2 = ld_c(ws, FL_OFF + lane * 16);
            BAR();
            wait_flags16(ws, FL_OFF + lane * 16, fl2, want);
            unsigned mu;
            for (unsigned s = 0; s < SPIN_CAP; ++s) {
                CBAR();
                mu = ld_c1(ws, SM_OFF + so);
                if (__all(mu != SENT)) break;
            }
            const float mv = asf(mu);
            const float M = max16(rowmax(mv));
            const float wf = __builtin_amdgcn_exp2f(mv - M);
            const unsigned wb = pk2(wf, wf);
            const unsigned keep = ((i16 >> 3) == (g >> 1)) ? hm : 0u;
            v4u af[4];
            {
                unsigned d0 = dppu<DPP_BC + 0>(wb) & keep, d1 = dppu<DPP_BC + 1>(wb) & keep, d2 = dppu<DPP_BC + 2>(wb) & keep, d3 = dppu<DPP_BC + 3>(wb) & keep;
#pragma unroll
                for (int q = 0; q < 4; ++q) {
                    af[0][q] = hq == q ? d0 : 0u; af[1][q] = hq == q ? d1 : 0u; af[2][q] = hq == q ? d2 : 0u; af[3][q] = hq == q ? d3 : 0u;
                }
                PIN(af[0]); PIN(af[1]); PIN(af[2]); PIN(af[3]);
            }
            BAR();
            STAMP(6);
            v4u o[4];
            unsigned lu;
            for (unsigned s = 0; s < SPIN_CAP; ++s) {
                CBAR();
#pragma unroll
                for (int j = 0; j < 4; ++j) o[j] = ld_c(ws, oo + j * 512);
                lu = ld_c1(ws, SL_OFF + so);
                if (__all(umax(umax(umax(mx4(o[0]), mx4(o[1])), umax(mx4(o[2]), mx4(o[3]))), lu) != SENT)) break;
            }
            STAMP(7);
            f32x4 d = Z4;
#pragma unroll
            for (int j = 0; j < 4; ++j) d = mfma32(af[j], o[j], d);
            const float Ls = add16(rowsum(bflo(wb) * asf(lu)));
            d *= __builtin_amdgcn_rcpf(Ls);
            const unsigned h0 = pk2(d[0], d[1]), h1 = pk2(d[2], d[3]);
            const unsigned l0 = pk2(d[0] - bflo(h0), d[1] - bfhi(h0)), l1 = pk2(d[2] - bflo(h1), d[3] - bfhi(h1));
            *(LAS v2u*)(L + clo) = v2u{h0, h1};
            *(LAS v2u*)(L + clo + 272) = v2u{l0, l1};
            BAR();
        } else {
            int pf[5];
#pragma unroll
            for (int i = 0; i < 4; ++i) {
                const int hh = 16 * hg + 4 * (w & 3) + i;
                pf[i] = OP_OFF + ((b * 32 + hh) * 4 + g) * 8192 + ((kb + 4 * hh + g) & 31) * 256 + i16 * 16;
                PIN(pf[i]);
            }
            {
                const int pc_ = CX_OFF + dh * 2048 + rg * 512 + dcq * 128 + c8 * 16;
                const int ps_ = SM_OFF + (lane >> 2) * 8192 + ((b * 32 + kb) * 32 + 16 * hg) * 4 + (lane & 3) * 16;
                pf[4] = lane < (w == 4 ? 32 : (w == 5 ? 8 : 0)) ? (w == 4 ? pc_ : ps_) : OOB;
                PIN(pf[4]);
            }
            SB();
            const void* wuvp = a.wuv;
            OPQ(wsp); OPQ(wuvp);
            const rsrc_t ws = mk(wsp, WS_BYTES);
#pragma unroll
            for (int i = 0; i < 5; ++i) st_c(ws, pf[i], SENT4);
            want = load_epoch(wsp);
            v4u fr[32];
            LAS unsigned char* tb = L + L_CK + (8 * g + tq) * 1024 + (w & 3) * 256 + (tp & 1) * 8;
            const int k0 = ((g & 1) << 3) | ((tq >> 1) << 2) | ((tq & 1) << 1), k1 = k0 ^ 9;
#define OFR(sg) _Pragma("unroll") for (int tau = 0; tau < 8; ++tau) \
                fr[tau * 4 + sg] = trd(tb + (((2 * tau + (tp >> 1)) ^ k0) * 16) + sg * 32768, tb + (((2 * tau + (tp >> 1)) ^ k1) * 16) + 4096 + sg * 32768);
            BAR(); OFR(0) BAR(); OFR(1) BAR(); OFR(2) fl = ld_c(ws, fo); BAR(); OFR(3)
            int ob[4];
#pragma unroll
            for (int rr = 0; rr < 4; ++rr) {
                const int hh = 16 * hg + 4 * g + rr;
                ob[rr] = OP_OFF + ((b * 32 + hh) * 4 + (w & 3)) * 8192 + ((kb + 4 * hh + (w & 3)) & 31) * 256 + i16 * 16;
                PIN(ob[rr]);
            }
            QGATHER();
            if (w == 7 && lane == 0) st_c2(ws, myfl, want);
            dma_loop(mks(wuvp, 512 * 4096 * 2), 8, rg * 8192 - (i16 & 8) * 16, i16, 0x66442200ull,
                     (128 * dcq + 64 * (w & 1)) * 8192 + dh * 256 + ((w >> 1) & 1) * 128, 8 * 8192, L_WV + (w & 3) * 8192);
            BAR();
            const f32x4 m4 = *(LAS f32x4*)(L + L_MX + i16 * 16);
            f32x4 o[8] = {Z4, Z4, Z4, Z4, Z4, Z4, Z4, Z4};
            f32x4 sv[8];
            v4u pp[4];
            LAS unsigned char* sp = L + L_S + i16 * SPITCH + g * 32;
#pragma unroll
            for (int i = 0; i < 8; ++i) sv[i] = *(LAS f32x4*)(sp + (i >> 1) * 128 + (i & 1) * 16);
            const float m = fmaxf(fmaxf(m4[0], m4[1]), fmaxf(m4[2], m4[3]));
#define PEXP(sg, q) pp[sg][q] = pk2(__builtin_amdgcn_exp2f(sv[2 * (sg) + ((q) >> 1)][2 * ((q) & 1)] - m), __builtin_amdgcn_exp2f(sv[2 * (sg) + ((q) >> 1)][2 * ((q) & 1) + 1] - m))
            PEXP(0, 0); PEXP(0, 1); PEXP(0, 2); PEXP(0, 3);
            SB();
#pragma unroll
            for (int sg = 0; sg < 4; ++sg) {
#pragma unroll
                for (int t2 = 0; t2 < 4; ++t2) {
                    o[2 * t2] = mfma32(pp[sg], fr[(2 * t2) * 4 + sg], o[2 * t2]);
                    o[2 * t2 + 1] = mfma32(pp[sg], fr[(2 * t2 + 1) * 4 + sg], o[2 * t2 + 1]);
                    if (sg < 3) PEXP(sg + 1, t2);
                    SB();
                }
            }
#pragma unroll
            for (int rr = 0; rr < 4; ++rr)
                st_c(ws, ob[rr], v4u{san(pk2(o[0][rr], o[1][rr])), san(pk2(o[2][rr], o[3][rr])), san(pk2(o[4][rr], o[5][rr])), san(pk2(o[6][rr], o[7][rr]))});
            if (w == 7) {
                f32x4 l = Z4;
#pragma unroll
                for (int sg = 0; sg < 4; ++sg) l = mfma32(ONES, pp[sg], l);
                st_c1(ws, lane < 16 ? SL_OFF + ((b * 32 + kb) * 32 + 16 * hg + i16) * 4 : OOB, asu(l[0]));
            }
            STAMP(5);
            BAR();
            VMW(4);
            BAR();
            v4u vf[16];
            {
                LAS unsigned char* vb = L + L_WV + (w & 1) * 16384 + (16 * tq + g) * 128 + tp * 8;
#pragma unroll
                for (int tau = 0; tau < 4; ++tau)
#pragma unroll
                    for (int s = 0; s < 4; ++s) {
                        LAS unsigned char* p = vb + ((tau ^ tq) * 32) + s * 512;
                        vf[tau * 4 + s] = trd(p, p + 8192);
                    }
            }
            int co = CX_OFF + dh * 2048 + (2 * (w & 1) + (g & 1)) * 512 + (2 * dcq + (g >> 1)) * 64 + i16 * 4;
            PIN(co);
            BAR();
            if (w < 6) {
                v4u af[4];
#pragma unroll
                for (int s = 0; s < 4; ++s) af[s] = *(LAS v4u*)(L + L_CL + i16 * 272 + (32 * s + 8 * g) * 2);
                f32x4 d[4] = {Z4, Z4, Z4, Z4};
#pragma unroll
                for (int s = 0; s < 4; ++s)
#pragma unroll
                    for (int tau = 0; tau < 4; ++tau) d[tau] = mfma32(af[s], vf[tau * 4 + s], d[tau]);
                const float e0 = rs16(sum4(d[0]), sum4(d[2])), e1 = rs16(sum4(d[1]), sum4(d[3]));
                st_c1(ws, co, san(pk2(e0, e1)));
            }
        }
        STAMP(8);
        DUMP();
        return;
    }

    const int ai = r - 16, h = dh, part = dcq;
    const bool isF = ai < 9;
    const int fu = 9 * xg + ai;
    const int e = ai - 9, kg = xg >> 1, et = (xg & 1) ? 5 + e : e;
    const bool isE = !isF && (e < ((xg & 1) ? 4 : 5));
    unsigned char* wsp = a.ws; const void* xp = a.x;
    if (w < 2) {
        __builtin_amdgcn_s_setprio(3);
        int xo = (lane >> 5) * 1152 + (lane & 31) * 36;
        int xe = ((i16 >> 3) * 576 + 144 * dcq + 72 * w + 18 * g) * 2;
        int qo = (144 * dcq + 72 * w + 18 * g) * 8192 + dh * 256 + i16 * 16;
        int qpo = QP_OFF + dh * 8192 + (i16 >> 2) * 2048 + (g & 1) * 1024 + ((g >> 1) * 8 + 2 * dcq + w) * 64 + (i16 & 3) * 16;
        PIN(xo); PIN(xe); PIN(qo); PIN(qpo);
        SB();
        const void* wqp = a.wq;
        OPQ(wsp); OPQ(xp); OPQ(wqp);
        const rsrc_t ws = mk(wsp, WS_BYTES);
        const rsrc_t rx = mk(xp, 2 * 576 * 2);
        const rsrc_t rq = mk(wqp, 576 * 4096 * 2);
        want = load_epoch(wsp);
        const v4u x0 = __builtin_amdgcn_raw_buffer_load_b128(rx, xo, 0, 0), x1 = __builtin_amdgcn_raw_buffer_load_b128(rx, xo + 16, 0, 0);
        const unsigned x2 = __builtin_amdgcn_raw_buffer_load_b32(rx, xo + 32, 0, 0);
        const v4u y0 = __builtin_amdgcn_raw_buffer_load_b128(rx, xe, 0, 0), y1 = __builtin_amdgcn_raw_buffer_load_b128(rx, xe + 16, 0, 0);
        const unsigned y2 = __builtin_amdgcn_raw_buffer_load_b32(rx, xe + 32, 0, 0);
        v4u wq[18];
#pragma unroll
        for (int j = 0; j < 18; ++j) wq[j] = __builtin_amdgcn_raw_buffer_load_b128(rq, qo, j * 8192, AUX_NT);
        SB();
        const unsigned xw[9] = {x0.x, x0.y, x0.z, x0.w, x1.x, x1.y, x1.z, x1.w, x2};
        const unsigned yw[9] = {y0.x, y0.y, y0.z, y0.w, y1.x, y1.y, y1.z, y1.w, y2};
        float s = 0.f;
#pragma unroll
        for (int q = 0; q < 9; ++q) s += bflo(xw[q]) + bfhi(xw[q]);
        float mean = add16(rowsum(s)) * (1.0f / 576.0f);
        float vs = 0.f;
#pragma unroll
        for (int q = 0; q < 9; ++q) { float d0 = bflo(xw[q]) - mean, d1 = bfhi(xw[q]) - mean; vs += d0 * d0; vs += d1 * d1; }
        float rs = __builtin_amdgcn_rsqf(add16(rowsum(vs)) * (1.0f / 576.0f) + 1e-5f) * (0.08838834764831845f * 1.4426950408889634f);
        unsigned sl[4], sh[4];
        v4u ones, af[18];
#pragma unroll
        for (int q = 0; q < 4; ++q) {
            sl[q] = hq == q ? ((i16 & 1) ? 0x01000c0cu : 0x0c0c0100u) : 0x0c0c0c0cu;
            sh[q] = hq == q ? ((i16 & 1) ? 0x03020c0cu : 0x0c0c0302u) : 0x0c0c0c0cu;
            ones[q] = hq == q ? (hm & 0x3F803F80u) : 0u;
        }
#pragma unroll
        for (int j = 0; j < 18; ++j) {
#pragma unroll
            for (int q = 0; q < 4; ++q) af[j][q] = __builtin_amdgcn_perm(0u, yw[j >> 1], (j & 1) ? sh[q] : sl[q]);
            PIN(af[j]);
        }
        PIN(ones); PIN(mean); PIN(rs);
        SB();
        STAMP(1);
        f32x4 acc = Z4, cs = Z4;
#pragma unroll
        for (int j = 0; j < 18; ++j) { acc = mfma32(af[j], wq[j], acc); cs = mfma32(ones, wq[j], cs); }
        const f32x4 v = (acc - cs * mean) * rs;
        st_c(ws, qpo, v4u{pk2(v[0], v[1]), want.x, pk2(v[2], v[3]), want.x});
        __builtin_amdgcn_s_setprio(0);
        STAMP(2);
    } else {
        if (w == 4 || w == 5) {
            const int wb = w - 4;
            int qa = QA_OFF + (((g >> 1) * 32 + h) * 512 + 128 * part + 64 * wb + 4 * i16 + 2 * (g & 1)) * 2;
            PIN(qa);
            SB();
            const void* wukp = a.wuk;
            OPQ(wsp); OPQ(wukp);
            const rsrc_t ws = mk(wsp, WS_BYTES);
            st_c1(ws, qa, SENT);
            want = load_epoch(wsp);
            __builtin_amdgcn_s_sleep(B_DELAY);
            dma_loop(mks(wukp, 512 * 4096 * 2), 16, g * 8192, i16, 0xFEDCBA9876543210ull, (128 * part + 64 * wb) * 8192 + h * 256, 4 * 8192,
                     L_UK + wb * 16384);
            VMW(16);
            if (lane == 0) st_c2(ws, FLQ_OFF + ((xg * 16 + ai) * 2 + wb) * 8, want);
            int po = QP_OFF + h * 8192 + i16 * 64 + g * 16;
            PIN(qa); PIN(po);
            VMW(1);
            STAMP(4);
            v4u qf[8];
#pragma unroll
            for (int j = 0; j < 8; ++j) qf[j] = ld_c(ws, po + j * 1024);
            v4u uk[16];
#pragma unroll
            for (int tau = 0; tau < 4; ++tau)
#pragma unroll
                for (int s = 0; s < 4; ++s) uk[tau * 4 + s] = *(LAS v4u*)(L + L_UK + wb * 16384 + i16 * 1024 + tau * 256 + (((4 * s + g) ^ i16) * 16));
            for (unsigned s = 0; s < SPIN_CAP; ++s) {
                unsigned dd = 0;
#pragma unroll
                for (int j = 0; j < 8; ++j) dd |= (qf[j].y ^ want.x) | (qf[j].w ^ want.x);
                if (__all(dd == 0u)) break;
                CBAR();
#pragma unroll
                for (int j = 0; j < 8; ++j) qf[j] = ld_c(ws, po + j * 1024);
            }
            STAMP(5);
            f32x4 d[4] = {Z4, Z4, Z4, Z4};
#pragma unroll
            for (int s = 0; s < 4; ++s) {
                const v4u q = {qf[2 * s].x, qf[2 * s].z, qf[2 * s + 1].x, qf[2 * s + 1].z};
#pragma unroll
                for (int tau = 0; tau < 4; ++tau) d[tau] = mfma32(q, uk[tau * 4 + s], d[tau]);
            }
            const float e0 = rs16(sum4(d[0]), sum4(d[2])), e1 = rs16(sum4(d[1]), sum4(d[3]));
            st_c1(ws, qa, san(pk2(e0, e1)));
            STAMP(6);
        } else {
            const int w3 = w < 4 ? w - 2 : w - 4;
            const int pf_ = FO_OFF + fu * 4608 + (w3 * 128 + lane) * 16;
            const int pe = AO_OFF + (2 * kg + rg) * 1152 + et * 128 + c8 * 16;
            const int lim = isF ? 288 - w3 * 128 : ((isE && w3 == 0) ? 16 : 0);
            int p0 = lane < lim ? (isF ? pf_ : pe) : OOB, p1 = lane + 64 < lim ? pf_ + 1024 : OOB;
            PIN(p0); PIN(p1);
            SB();
            OPQ(wsp);
            const rsrc_t ws = mk(wsp, WS_BYTES);
            st_c(ws, p0, SENT4);
            st_c(ws, p1, SENT4);
            want = load_epoch(wsp);
            VMW(0);
        }
    }
    OPQ(want);
    const rsrc_t ws = mk(wsp, WS_BYTES);
    const rsrc_t rx = mk(xp, 2 * 576 * 2);
    BAR();
    if (w == 7 && lane == 0) st_c2(ws, myfl, want);
    __builtin_amdgcn_s_sleep(1);

    if (isF) {
        for (int z_ = 0; z_ < F_DELAY; ++z_) __builtin_amdgcn_s_sleep(8);
        {
            const int key = ((rg >> 1) & 1) | ((w & 1) << 1);
            dma_loop(mks(a.wd, 4608 * 576 * 2), 9, rg * 1152 - (i16 & 8) * 16, i16 ^ (key << 1), 0ull, (64 * fu + 8 * w) * 1152, 128, L_DN + w * 1024, 8192);
        }
        const int keyg = (((w & 3) >> 1) & 1) | (((rg >> 2) & 1) << 1);
        const v4u sgu = mks((w >> 2) ? a.wu : a.wg, 576 * 4608 * 2);
        const int sgo = 8 * (w & 3) * 9216 + fu * 128;
        dma_loop(sgu, 10, rg * 9216 - (i16 & 8) * 16, i16 ^ (keyg << 1), 0ull, sgo, 32 * 9216, L_GU1 + w * 1024, 8192);
        VMW(10);
        BAR();
        v4u df[10];
        {
            const int key = ((tq >> 1) & 1) | ((g & 1) << 1);
            LAS unsigned char* tb = L + L_DN + (w >> 2) * 8192 + (8 * g + tq) * 128 + tp * 8 + (((w & 3) ^ key) * 32);
#pragma unroll
            for (int i = 0; i < 5; ++i)
#pragma unroll
                for (int s = 0; s < 2; ++s) {
                    LAS unsigned char* p = tb + (i < 4 ? i : (w < 4 ? 4 : 0)) * 16384 + s * 4096;
                    df[i * 2 + s] = trd(p, p + 512);
                }
        }
        BAR();
        dma_loop(sgu, 8, rg * 9216 - (i16 & 8) * 16, i16 ^ (keyg << 1), 0ull, sgo + 10 * 32 * 9216, 32 * 9216, L_GU2 + w * 1024, 8192);
        VMW(8);
        BAR();
        v4u gf[18], uf[18];
        const int keyp = (tq & 1) | ((g & 1) << 1);
        const int tro = (16 * (tq & 1) + 4 * g + (tq >> 1)) * 128 + tp * 8 + (((w & 3) ^ keyp) * 32);
        if (w < 4) {
#pragma unroll
            for (int s = 0; s < 10; ++s) {
                LAS unsigned char* p = L + L_GU1 + s * 8192 + tro;
                gf[s] = trd(p, p + 256);
                uf[s] = trd(p + 4096, p + 4096 + 256);
            }
        }
        BAR();
        if (w >= 4) {
            const int t = tid - 256;
#pragma unroll
            for (int i = 0; i < 3; ++i) {
                const int k = t + 256 * i;
                if (k < 576) {
                    const int bb = k >= 288, kk = k - 288 * bb, m0 = bb * 576 + (kk >> 4) * 32 + (kk & 15);
                    const unsigned lo = __builtin_amdgcn_raw_buffer_load_b16(rx, m0 * 2, 0, 0), hi = __builtin_amdgcn_raw_buffer_load_b16(rx, (m0 + 16) * 2, 0, 0);
                    *(LAS unsigned*)(L + L_AO + (8 + bb) * AOP + kk * 4) = lo | (hi << 16);
                }
            }
#pragma unroll
            for (int i = 0; i < 2; ++i) {
                const int k = t + 256 * i;
                if (k < 6 * 73) *(LAS v4u*)(L + L_AO + 10 * AOP + k * 16) = v4u{0u, 0u, 0u, 0u};
            }
            if (w == 6) check_flags(ws, want, lane);
        }
        VMW(0);
        BAR();
        STAMP(7);
        float csg = 0.f, csu = 0.f;
        if (w < 4) {
#pragma unroll
            for (int s = 10; s < 18; ++s) {
                LAS unsigned char* p = L + L_GU2 + (s - 10) * 8192 + tro;
                gf[s] = trd(p, p + 256);
                uf[s] = trd(p + 4096, p + 4096 + 256);
            }
            f32x4 cg = Z4, cu = Z4;
#pragma unroll
            for (int s = 0; s < 18; ++s) { cg = mfma32(ONES, gf[s], cg); cu = mfma32(ONES, uf[s], cu); }
            csg = cg[0]; csu = cu[0];
            PIN(csg); PIN(csu);
        } else {
            const int wq_ = w - 4;
            int go[3], lo_[3];
#pragma unroll
            for (int i = 0; i < 3; ++i) {
                const int id = i * 64 + lane;
                go[i] = id < 144 ? AO_OFF + wq_ * 2304 + id * 16 : OOB;
                lo_[i] = id < 144 ? L_AO + wq_ * 2 * AOP + id * 16 + (id >= 72 ? 16 : 0) : L_JUNK + lane * 16;
                PIN(lo_[i]);
            }
            v4u av[3];
            for (unsigned s = 0; s < SPIN_CAP; ++s) {
                CBAR();
#pragma unroll
                for (int i = 0; i < 3; ++i) av[i] = ld_c(ws, go[i]);
                if (__all(umax(umax(mx4(av[0]), mx4(av[1])), mx4(av[2])) != SENT)) break;
            }
            STAMP(8);
#pragma unroll
            for (int i = 0; i < 3; ++i) *(LAS v4u*)(L + lo_[i]) = av[i];
        }
        int fo = i16 < 2 ? FO_OFF + fu * 4608 + w * 128 + i16 * 64 + g * 16 : OOB;
        PIN(fo);
        BAR();
        LAS unsigned char* ab = L + L_AO + i16 * AOP + g * 16;
        f32x4 dg = Z4, du = Z4;
        if (w < 4) {
            v4u af[6];
#pragma unroll
            for (int s = 0; s < 6; ++s) af[s] = *(LAS v4u*)(ab + s * 64);
            SB();
#pragma unroll
            for (int s = 0; s < 18; ++s) {
                const v4u cur = af[s % 6];
                dg = mfma32(cur, gf[s], dg);
                du = mfma32(cur, uf[s], du);
                if (s + 6 < 18) af[s % 6] = *(LAS v4u*)(ab + (s + 6) * 64);
                SB();
            }
        } else {
            const int wq_ = w - 4;
            v4u af[5];
#pragma unroll
            for (int i = 0; i < 5; ++i) af[i] = *(LAS v4u*)(ab + (i < 4 || wq_ < 2 ? wq_ + 4 * i : 0) * 64);
#pragma unroll
            for (int i = 0; i < 5; ++i) {
                if (i < 4 || wq_ < 2) {
                    dg = mfma32(af[i], af[i], dg);
                    du = mfma32(af[i], ONES, du);
                }
            }
            float s2 = (i16 & 1) ? dg[1] + dg[3] : dg[0] + dg[2];
            const float s1 = (i16 & 1) ? du[1] + du[3] : du[0] + du[2];
            s2 += dpp<DPP_ROR8>(s2); s2 += dpp<DPP_ROR4>(s2); s2 += dpp<DPP_ROR2>(s2);
            const float st = add32(rs16(s1, s2));
            if (lane < 32 && i16 < 2) *(LAS float*)(L + L_FS + ((i16 * 2 + g) * 4 + wq_) * 4) = st;
        }
        BAR();
        if (w < 4) {
            const f32x4 s1 = *(LAS f32x4*)(L + L_FS + (g >> 1) * 32), s2 = *(LAS f32x4*)(L + L_FS + (g >> 1) * 32 + 16);
            const float gs = add16(rs32(dg[0] + dg[2], dg[1] + dg[3])), us = add16(rs32(du[0] + du[2], du[1] + du[3]));
            const float mean = sum4(s1) * (1.0f / 576.0f);
            const float var = fmaxf(sum4(s2) * (1.0f / 576.0f) - mean * mean, 0.f);
            const float rstd = __builtin_amdgcn_rsqf(var + 1e-5f);
            const float gg = (gs - mean * csg) * rstd, uu = (us - mean * csu) * rstd;
            const float act = gg * uu * __builtin_amdgcn_rcpf(1.0f + __builtin_amdgcn_exp2f(gg * -1.4426950408889634f));
            if ((g & 1) == 0) *(LAS unsigned short*)(L + L_ACT + (g >> 1) * 128 + (16 * w + i16) * 2) = (unsigned short)pk2(act, act);
        }
        BAR();
        {
            const v4u b0 = *(LAS v4u*)(L + L_ACT + (i16 & 1) * 128 + g * 16), b1 = *(LAS v4u*)(L + L_ACT + (i16 & 1) * 128 + 64 + g * 16);
#pragma unroll
            for (int i = 0; i < 5; ++i) {
                if (i < 4 || w < 4) {
                    f32x4 d = mfma32(df[2 * i + 1], b1, mfma32(df[2 * i], b0, Z4));
                    st_c(ws, fo + i * 1024, __builtin_bit_cast(v4u, d));
                }
            }
        }
        STAMP(9);
        DUMP();
        return;
    }
    if (!isE) {
        if (blockIdx.x == 255 && w == 6) {
            check_flags(ws, want, lane);
            if (lane == 0) st_c2(ws, EPOCH_OFF, want);
        }
        DUMP();
        return;
    }
    {
        const int hh = 8 * kg + w;
        for (int z_ = 0; z_ < E_DELAY; ++z_) __builtin_amdgcn_s_sleep(8);
        dma_loop(mks(a.wo, 4096 * 576 * 2), 16, rg * 1152 - (i16 & 8) * 16, i16 ^ (((rg >> 2) & 1) << 2), 0x2200220022002200ull,
                 hh * 128 * 1152 + et * 128, 8 * 1152, L_WO + w * 16384);
        if (w == 6) check_flags(ws, want, lane);
        VMW(0);
        v4u wf[16];
        {
            const int keyp = (tq & 1) | ((g & 1) << 1);
            LAS unsigned char* tb = L + L_WO + w * 16384 + (16 * (tq & 1) + 4 * g + (tq >> 1)) * 128 + tp * 8;
#pragma unroll
            for (int tau = 0; tau < 4; ++tau)
#pragma unroll
                for (int s = 0; s < 4; ++s) {
                    LAS unsigned char* p = tb + ((tau ^ keyp) * 32) + s * 4096;
                    wf[tau * 4 + s] = trd(p, p + 256);
                }
        }
        int ao = AO_OFF + (2 * kg + (lane >> 5)) * 1152 + et * 128 + (lane & 31) * 4;
        PIN(ao);
        BAR();
        STAMP(7);
        v4u cf[4];
        const int co = i16 < 8 ? CX_OFF + hh * 2048 + i16 * 64 + g * 16 : OOB;
        for (unsigned s = 0; s < SPIN_CAP; ++s) {
            CBAR();
#pragma unroll
            for (int j = 0; j < 4; ++j) cf[j] = ld_c(ws, co + j * 512);
            if (__all(umax(umax(mx4(cf[0]), mx4(cf[1])), umax(mx4(cf[2]), mx4(cf[3]))) != SENT)) break;
        }
        STAMP(8);
        {
            f32x4 d[4] = {Z4, Z4, Z4, Z4};
#pragma unroll
            for (int s = 0; s < 4; ++s)
#pragma unroll
                for (int tau = 0; tau < 4; ++tau) d[tau] = mfma32(cf[s], wf[tau * 4 + s], d[tau]);
            const f32x4 ev = {rs16(d[0][0] + d[0][2], d[2][0] + d[2][2]), rs16(d[1][0] + d[1][2], d[3][0] + d[3][2]),
                              rs16(d[0][1] + d[0][3], d[2][1] + d[2][3]), rs16(d[1][1] + d[1][3], d[3][1] + d[3][3])};
            if (lane < 32) *(LAS f32x4*)(L + L_RE + w * 512 + lane * 16) = ev;
        }
        BAR();
        const int ru = 9 * kg + et;
        if (w == 0) {
            LAS unsigned char* p = L + L_RE + (lane >> 5) * 2048 + (lane & 31) * 16;
            const f32x4 v = (*(LAS f32x4*)p + *(LAS f32x4*)(p + 512)) + (*(LAS f32x4*)(p + 1024) + *(LAS f32x4*)(p + 1536));
            st_c1(ws, ao, san(pk2(rs32(v[0], v[2]), rs32(v[1], v[3]))));
            STAMP(9);
        } else if (w == 1) {
            const v2u xr = __builtin_amdgcn_raw_buffer_load_b64(rx, rg == 4 ? ((c8 >> 2) * 576 + 16 * ru + 4 * (c8 & 3)) * 2 : OOB, 0, 0);
            __builtin_amdgcn_s_sleep(R_DELAY);
            v4u hv;
            const int ho = rg < 4 ? AO_OFF + (2 * rg + (c8 >> 2)) * 1152 + (ru >> 1) * 64 + (c8 & 3) * 16 : OOB;
            for (unsigned s = 0; s < SPIN_CAP; ++s) {
                CBAR();
                hv = ld_c(ws, ho);
                if (__all(mx4(hv) != SENT)) break;
            }
            f32x4 acc = {bflo(xr.x), bfhi(xr.x), bflo(xr.y), bfhi(xr.y)};
#pragma unroll
            for (int j = 0; j < 4; ++j) acc[j] += (ru & 1) ? bfhi(hv[j]) : bflo(hv[j]);
            PIN(acc);
            v4u fv[9];
            const int fo = FO_OFF + rg * 4608 + ru * 128 + c8 * 16;
            for (unsigned s = 0; s < SPIN_CAP; ++s) {
                CBAR();
#pragma unroll
                for (int j = 0; j < 9; ++j) fv[j] = ld_c(ws, fo + j * 8 * 4608);
                unsigned m = mx4(fv[0]);
#pragma unroll
                for (int j = 1; j < 9; ++j) m = umax(m, mx4(fv[j]));
                if (__all(m != SENT)) break;
            }
            STAMP(10);
#pragma unroll
            for (int j = 0; j < 9; ++j) acc += __builtin_bit_cast(f32x4, fv[j]);
            float u0 = rs32(acc[0], acc[2]), u1 = rs32(acc[1], acc[3]);
            u0 = add16(u0); u1 = add16(u1);
            u0 += dpp<DPP_ROR8>(u0); u1 += dpp<DPP_ROR8>(u1);
            if ((lane & 31) < 8)
                *(unsigned*)((unsigned char*)a.out + ((c8 >> 2) * 576 + 16 * ru + 4 * (c8 & 3) + 2 * (lane >> 5)) * 2) = pk2(u0, u1);
            STAMP(11);
        }
        DUMP();
    }
}

}

extern "C" void kernel_launch(void* const* d_in, const int* in_sizes, int n_in, void* d_out, int out_size, void* d_ws,
                              size_t ws_size, hipStream_t stream) {
    Args a;
    a.x = d_in[0]; a.ckv = d_in[1]; a.wq = d_in[2]; a.wuk = d_in[3]; a.wuv = d_in[4]; a.wo = d_in[5];
    a.wg = d_in[7]; a.wu = d_in[8]; a.wd = d_in[9];
    a.out = d_out; a.ws = (unsigned char*)d_ws;
    (void)hipFuncSetAttribute((const void*)fused, hipFuncAttributeMaxDynamicSharedMemorySize, LDS_BYTES);
    hipLaunchKernelGGL(fused, dim3(256), dim3(NT), LDS_BYTES, stream, a);
}
```

```cpp
#include <hip/hip_runtime.h>
#include <hip/hip_bf16.h>
#include <cstdio>

#define LAS __attribute__((address_space(3)))
typedef unsigned v4u __attribute__((ext_vector_type(4)));
typedef unsigned v2u __attribute__((ext_vector_type(2)));
typedef float f32x4 __attribute__((ext_vector_type(4)));
typedef float f32x2 __attribute__((ext_vector_type(2)));
typedef short s16x4 __attribute__((ext_vector_type(4)));
typedef __bf16 bf16x8 __attribute__((ext_vector_type(8)));
typedef __bf16 bf16x2 __attribute__((ext_vector_type(2)));
typedef __amdgpu_buffer_rsrc_t rsrc_t;
#define DI __device__ __forceinline__

#define STAMPS 0

#pragma clang diagnostic ignored "-Winline-asm"
#define KVER 10
namespace {

constexpr int NT = 512, NW = 8;
constexpr unsigned SENT = 0xFFFFFFFFu;
constexpr int AUX_NT = 2, AUX_SC1 = 16;
constexpr unsigned SPIN_CAP = 1u << 18;
constexpr int OOB = 0x7ffff000;

constexpr int EPOCH_OFF = 0;
constexpr int FL_OFF = 1024;
constexpr int FLQ_OFF = 4096;
constexpr int QP_OFF = 8192;
constexpr int QA_OFF = QP_OFF + 262144;
constexpr int SM_OFF = QA_OFF + 65536;
constexpr int SL_OFF = SM_OFF + 8192;
constexpr int CX_OFF = SL_OFF + 8192;
constexpr int AO_OFF = CX_OFF + 65536;
constexpr int FO_OFF = AO_OFF + 9216;
constexpr int OP_OFF = 1 << 20;
constexpr int WS_BYTES = OP_OFF + (2 << 20);
constexpr int DBG_OFF = 8 << 20;

constexpr int L_CK = 0;
constexpr int L_Q = 131072;
constexpr int SPITCH = 528;
constexpr int L_S = L_Q + 16384;
constexpr int L_MX = L_S + 16 * SPITCH;
constexpr int L_WV = 0;
constexpr int L_CL = L_Q;
constexpr int L_UK = 0;
constexpr int L_DN = 0;
constexpr int L_GU1 = 73728;
constexpr int L_GU2 = 0;
constexpr int L_AO = 73728;
constexpr int AOP = 1168;
constexpr int L_FS = 155648;
constexpr int L_ACT = L_FS + 64;
constexpr int L_WO = 0;
constexpr int L_RE = 131072;
constexpr int L_DBG = 156672;
constexpr int L_JUNK = L_DBG + 1024;
constexpr int LDS_BYTES = L_JUNK + 1024;
static_assert(L_MX + 256 <= L_DBG && L_ACT + 256 <= L_DBG, "lds map");
__host__ __device__ constexpr int ckey(int row) { return ((((row >> 3) ^ (row >> 2)) & 1) << 3) | (((row >> 1) & 1) << 2) | ((row & 1) << 1) | ((row >> 2) & 1); }
constexpr unsigned long long ckey_tab() { unsigned long long t = 0; for (int i = 0; i < 16; ++i) t |= (unsigned long long)ckey(i) << (4 * i); return t; }

DI v4u ld_nt(rsrc_t r, int off) { return __builtin_amdgcn_raw_buffer_load_b128(r, off, 0, AUX_NT); }
DI v4u ld_c(rsrc_t r, int off) { return __builtin_amdgcn_raw_buffer_load_b128(r, off, 0, AUX_SC1); }
DI v2u ld_c2(rsrc_t r, int off) { return __builtin_amdgcn_raw_buffer_load_b64(r, off, 0, AUX_SC1); }
DI unsigned ld_c1(rsrc_t r, int off) { return __builtin_amdgcn_raw_buffer_load_b32(r, off, 0, AUX_SC1); }
DI void st_c(rsrc_t r, int off, v4u v) { __builtin_amdgcn_raw_buffer_store_b128(v, r, off, 0, AUX_SC1); }
DI void st_c2(rsrc_t r, int off, v2u v) { __builtin_amdgcn_raw_buffer_store_b64(v, r, off, 0, AUX_SC1); }
DI void st_c1(rsrc_t r, int off, unsigned v) { __builtin_amdgcn_raw_buffer_store_b32(v, r, off, 0, AUX_SC1); }
DI unsigned san(unsigned x) { return x < 0xFFFFFFFEu ? x : 0xFFFFFFFEu; }
DI rsrc_t mk(const void* p, unsigned bytes) { return __builtin_amdgcn_make_buffer_rsrc((void*)p, 0, bytes, 0x00020000); }
DI v4u mks(const void* p, unsigned bytes) {
    const unsigned long long a = (unsigned long long)p;
    return v4u{(unsigned)a, (unsigned)(a >> 32) & 0xFFFFu, bytes, 0x00020000u};
}
DI void dma(v4u srd, int voff, int soff, int lds) {
    asm volatile("s_mov_b32 m0, %0\n\ts_nop 0\n\tbuffer_load_dwordx4 %1, %2, %3 offen nt lds" ::"s"(lds), "v"(voff), "s"(srd), "s"(soff) : "m0", "memory");
}
DI void dmap(v4u srd, int voff, int soff, int lds) {
    asm volatile("s_mov_b32 m0, %0\n\ts_nop 0\n\tbuffer_load_dwordx4 %1, %2, %3 offen lds" ::"s"(lds), "v"(voff), "s"(srd), "s"(soff) : "m0", "memory");
}
DI float asf(unsigned u) { return __builtin_bit_cast(float, u); }
DI unsigned asu(float f) { return __builtin_bit_cast(unsigned, f); }
DI float bflo(unsigned u) { return asf(u << 16); }
DI float bfhi(unsigned u) { return asf(u & 0xFFFF0000u); }
DI unsigned pk2(float a, float b) { bf16x2 t = {(__bf16)a, (__bf16)b}; return __builtin_bit_cast(unsigned, t); }
DI float dot2(unsigned a, unsigned b, float c) { return __builtin_amdgcn_fdot2_f32_bf16(__builtin_bit_cast(bf16x2, a), __builtin_bit_cast(bf16x2, b), c, false); }
DI f32x4 mfma32(v4u a, v4u b, f32x4 c) {
    return __builtin_amdgcn_mfma_f32_16x16x32_bf16(__builtin_bit_cast(bf16x8, a), __builtin_bit_cast(bf16x8, b), c, 0, 0, 0);
}
template <int CTRL> DI float dpp(float v) { return asf((unsigned)__builtin_amdgcn_update_dpp(0, (int)asu(v), CTRL, 0xf, 0xf, true)); }
template <int CTRL> DI unsigned dppu(unsigned v) { return (unsigned)__builtin_amdgcn_update_dpp(0, (int)v, CTRL, 0xf, 0xf, true); }
constexpr int DPP_X1 = 0xB1, DPP_X2 = 0x4E, DPP_HMIR = 0x141, DPP_ROR8 = 0x128, DPP_ROR4 = 0x124, DPP_ROR2 = 0x122, DPP_ROR1 = 0x121;
constexpr int DPP_BC = 0x150;
DI float rs16(float a, float b) { auto r = __builtin_amdgcn_permlane16_swap(asu(a), asu(b), false, false); unsigned x = r[0], y = r[1]; return asf(x) + asf(y); }
DI float rs32(float a, float b) { auto r = __builtin_amdgcn_permlane32_swap(asu(a), asu(b), false, false); unsigned x = r[0], y = r[1]; return asf(x) + asf(y); }
DI float add16(float v) { return rs16(v, v); }
DI float add32(float v) { return rs32(v, v); }
DI float max16(float v) { auto r = __builtin_amdgcn_permlane16_swap(asu(v), asu(v), false, false); unsigned a = r[0], b = r[1]; return fmaxf(asf(a), asf(b)); }
DI float max32(float v) { auto r = __builtin_amdgcn_permlane32_swap(asu(v), asu(v), false, false); unsigned a = r[0], b = r[1]; return fmaxf(asf(a), asf(b)); }
DI float rowsum(float v) { v += dpp<DPP_ROR8>(v); v += dpp<DPP_ROR4>(v); v += dpp<DPP_ROR2>(v); v += dpp<DPP_ROR1>(v); return v; }
DI float rowmax(float v) { v = fmaxf(v, dpp<DPP_ROR8>(v)); v = fmaxf(v, dpp<DPP_ROR4>(v)); v = fmaxf(v, dpp<DPP_ROR2>(v)); v = fmaxf(v, dpp<DPP_ROR1>(v)); return v; }
DI float sum4(f32x4 d) { return (d[0] + d[2]) + (d[1] + d[3]); }
DI unsigned umax(unsigned a, unsigned b) { return a > b ? a : b; }
DI unsigned mx4(v4u v) { return umax(umax(v.x, v.y), umax(v.z, v.w)); }
DI v4u trd(LAS unsigned char* p0, LAS unsigned char* p1) {
    v2u a = __builtin_bit_cast(v2u, __builtin_amdgcn_ds_read_tr16_b64_v4i16((LAS s16x4*)p0));
    v2u b = __builtin_bit_cast(v2u, __builtin_amdgcn_ds_read_tr16_b64_v4i16((LAS s16x4*)p1));
    return v4u{a.x, a.y, b.x, b.y};
}
#define CBAR() asm volatile("" ::: "memory")
#define VMW(n) asm volatile("s_waitcnt vmcnt(" #n ")" ::: "memory")
#define BAR() asm volatile("s_waitcnt lgkmcnt(0)\n\ts_barrier" ::: "memory")
#define PIN(v) asm volatile("" : "+v"(v))
#define SB() __builtin_amdgcn_sched_barrier(0)
#define OPQ(p) asm volatile("" : "+s"(p))
constexpr f32x4 Z4 = {0.f, 0.f, 0.f, 0.f};

DI void check_flags(rsrc_t ws, v2u want, int lane) {
    for (unsigned s = 0; s < SPIN_CAP; ++s) {
        CBAR();
        v4u a = ld_c(ws, FL_OFF + lane * 16), b = ld_c(ws, FL_OFF + 1024 + lane * 16);
        unsigned d = (a.x ^ want.x) | (a.y ^ want.y) | (a.z ^ want.x) | (a.w ^ want.y) | (b.x ^ want.x) | (b.y ^ want.y) | (b.z ^ want.x) | (b.w ^ want.y);
        if (__all(d == 0u)) break;
    }
    CBAR();
}
DI v2u load_epoch(const unsigned char* ws) {
    const unsigned long long e = *(const __attribute__((address_space(4))) unsigned long long*)(ws + EPOCH_OFF) + 1ull;
    return v2u{(unsigned)e, (unsigned)(e >> 32)};
}

#define STAMP(i) do {} while (0)
#define DUMP() do {} while (0)
#define STAMP_INIT() do {} while (0)

struct Args {
    const void *x, *ckv, *wq, *wuk, *wuv, *wo, *wg, *wu, *wd;
    void* out;
    unsigned char* ws;
};
#define G1_DELAY 0
#define B_DELAY 10
#define E_DELAY 6
#define F_DELAY 3
#define WUV_EARLY 0
#define NO_PREFILL 0
#define R_DELAY 20
#define CODE_KB 19

DI void dma_loop(v4u srd, int n, int vhi, int i16, unsigned long long tab, int soff, int sstep, int lds, int lstep = 1024) {
    soff = __builtin_amdgcn_readfirstlane(soff); lds = __builtin_amdgcn_readfirstlane(lds);
    asm volatile("s_nop 4" : "+s"(soff), "+s"(lds));
#pragma unroll 1
    for (int i = 0; i < n; ++i) {
        dma(srd, vhi + ((i16 ^ (int)((unsigned)tab & 15u)) << 4), soff, lds);
        tab >>= 4; soff += sstep; lds += lstep;
    }
}
DI void wait_flags16(rsrc_t ws, int off, v4u fl, v2u want) {
    for (unsigned s = 0; s < SPIN_CAP; ++s) {
        if (__all(((fl.x ^ want.x) | (fl.y ^ want.y) | (fl.z ^ want.x) | (fl.w ^ want.y)) == 0u)) break;
        CBAR();
        fl = ld_c(ws, off);
    }
    CBAR();
}

__global__ void __launch_bounds__(NT, 2) fused(Args a) {
    extern __shared__ __attribute__((aligned(16))) unsigned char smem[];
    LAS unsigned char* L = (LAS unsigned char*)smem;
    const int tid = threadIdx.x, lane = tid & 63, w = __builtin_amdgcn_readfirstlane(tid >> 6);
    const int xg = blockIdx.x & 7, r = blockIdx.x >> 3;
    const int c8 = lane & 7, rg = lane >> 3, i16 = lane & 15, g = lane >> 4;
    const int tq = i16 >> 2, tp = i16 & 3;
    const v4u SENT4 = {SENT, SENT, SENT, SENT};
    const int dh = 4 * xg + ((r & 15) >> 2), dcq = r & 3;
    const unsigned hm = (i16 & 1) ? 0xFFFF0000u : 0x0000FFFFu;
    const int hq = (i16 >> 1) & 3;
    const v4u ONES = {0x3F803F80u, 0x3F803F80u, 0x3F803F80u, 0x3F803F80u};
    if (w == 7 && r < CODE_KB) {
        unsigned long long pc;
        asm volatile("s_getpc_b64 %0" : "=s"(pc));
        pc &= ~0xFFull;
        const v4u sc = v4u{(unsigned)pc, (unsigned)(pc >> 32) & 0xFFFFu, (unsigned)(CODE_KB * 1024), 0x00020000u};
        dmap(sc, lane * 16, r * 1024, L_JUNK);
    }
    STAMP_INIT();
    STAMP(0);
    v2u want;

    const int myfl = FL_OFF + ((r >> 4) * 128 + xg * 16 + (r & 15)) * 8;
    if (r < 16) {
        const int b = xg >> 2, kb = 8 * (xg & 3) + (r >> 1), hg = r & 1;
        unsigned char* wsp = a.ws;
        const int fo = FLQ_OFF + (w * 16 + i16) * 16;
        v4u fl;
#define QGATHER() \
        int ql0 = L_Q + w * 1024 + ((lane ^ ckey(w)) * 16), ql1 = L_Q + (8 + w) * 1024 + ((lane ^ ckey(8 + w)) * 16); \
        int qoff = QA_OFF + (b * 32 + 16 * hg) * 1024 + tid * 16; \
        PIN(ql0); PIN(ql1); PIN(qoff); \
        VMW(0); \
        OPQ(want); \
        STAMP(1); \
        wait_flags16(ws, fo, fl, want); \
        BAR(); \
        STAMP(2); \
        v4u q0, q1; \
        for (unsigned s = 0; s < SPIN_CAP; ++s) { \
            CBAR(); \
            q0 = ld_c(ws, qoff); q1 = ld_c(ws, qoff + 8192); \
            if (__all(umax(mx4(q0), mx4(q1)) != SENT)) break; \
        } \
        STAMP(3); \
        *(LAS v4u*)(L + ql0) = q0; \
        *(LAS v4u*)(L + ql1) = q1; \
        BAR();
        if (w < 4) {
            const int key = ckey(i16);
            v4u fr[32];
            const unsigned long long tab = (w & 1) ? (ckey_tab() >> 32) : ckey_tab();
            const int cso = (b * 4096 + 128 * kb + 8 * w) * 1024;
            int vhi = g * 256;
            PIN(vhi);
            SB();
            const void* ckvp = a.ckv;
            OPQ(wsp); OPQ(ckvp);
            const rsrc_t ws = mk(wsp, WS_BYTES);
            const v4u sck = mks(ckvp, 2 * 4096 * 512 * 2);
#define CKV(sg) dma_loop(sck, 8, vhi, i16, tab, cso + sg * 32768, 1024, L_CK + sg * 32768 + w * 8192)
#define SFR() do { LAS unsigned char* sb = L + L_CK + (32 * w + i16) * 1024; \
                _Pragma("unroll") for (int s = 0; s < 16; ++s) { \
                    LAS unsigned char* p = sb + ((((4 * s + g) & 15) ^ key) * 16) + (s >> 2) * 256; \
                    fr[s] = *(LAS v4u*)p; fr[16 + s] = *(LAS v4u*)(p + 16384); } } while (0)
#define LSTEP0() do { VMW(16); BAR(); CKV(3); } while (0)
#define LSTEP1() do { VMW(16); BAR(); } while (0)
#define LSTEP2() do { VMW(8); BAR(); } while (0)
#define LSTEP3() do { fl = ld_c(ws, fo); VMW(1); BAR(); } while (0)
            __builtin_amdgcn_s_sleep(G1_DELAY);
            CKV(0); CKV(1); CKV(2);
            want = load_epoch(wsp);
            LSTEP0();
            if (w > 0) { LSTEP1(); if (w > 1) { LSTEP2(); if (w > 2) LSTEP3(); } }
            SFR();
            if (w < 1) LSTEP1();
            if (w < 2) LSTEP2();
            if (w < 3) LSTEP3();
            QGATHER();
            f32x4 s0 = Z4, s1 = Z4;
            LAS unsigned char* qb = L + L_Q + i16 * 1024;
            v4u qf[16];
#pragma unroll
            for (int s = 0; s < 16; ++s) qf[s] = *(LAS v4u*)(qb + ((((4 * s + g) & 15) ^ key) * 16) + (s >> 2) * 256);
            SB();
#pragma unroll
            for (int s = 0; s < 16; ++s) {
                s0 = mfma32(fr[s], qf[s], s0);
                s1 = mfma32(fr[16 + s], qf[s], s1);
            }
            LAS unsigned char* sp = L + L_S + i16 * SPITCH + (32 * w + 4 * g) * 4;
            *(LAS f32x4*)sp = s0;
            *(LAS f32x4*)(sp + 64) = s1;
            float mx = fmaxf(fmaxf(fmaxf(s0[0], s0[1]), fmaxf(s0[2], s0[3])), fmaxf(fmaxf(s1[0], s1[1]), fmaxf(s1[2], s1[3])));
            mx = max32(max16(mx));
            if (lane < 16) *(LAS float*)(L + L_MX + (i16 * 4 + w) * 4) = mx;
            STAMP(4);
            BAR();
            if (w == 0) {
                const f32x4 m4 = *(LAS f32x4*)(L + L_MX + i16 * 16);
                const float m = fmaxf(fmaxf(m4[0], m4[1]), fmaxf(m4[2], m4[3]));
                st_c1(ws, lane < 16 ? SM_OFF + ((b * 32 + kb) * 32 + 16 * hg + i16) * 4 : OOB, asu(m));
            }
            const int kbl = (2 * ((i16 + 4 * w) & 15) + (g & 1) - 4 * dh - dcq) & 31;
            int so = ((g >> 1) * 32 + kbl) * 128 + dh * 4;
            int oo = OP_OFF + (((g >> 1) * 32 + dh) * 4 + dcq) * 8192 + (8 * w + (g & 1)) * 256 + i16 * 16;
            int clo = L_CL + ((g >> 1) * 8 + 2 * w) * 272 + (8 * i16 + 4 * (g & 1)) * 2;
            PIN(clo); PIN(so); PIN(oo);
            __builtin_amdgcn_s_sleep(4);
            v4u fl2 = ld_c(ws, FL_OFF + lane * 16);
            BAR();
            wait_flags16(ws, FL_OFF + lane * 16, fl2, want);
            unsigned mu;
            for (unsigned s = 0; s < SPIN_CAP; ++s) {
                CBAR();
                mu = ld_c1(ws, SM_OFF + so);
                if (__all(mu != SENT)) break;
            }
            const float mv = asf(mu);
            const float M = max16(rowmax(mv));
            const float wf = __builtin_amdgcn_exp2f(mv - M);
            const unsigned wb = pk2(wf, wf);
            const unsigned keep = ((i16 >> 3) == (g >> 1)) ? hm : 0u;
            v4u af[4];
            {
                unsigned d0 = dppu<DPP_BC + 0>(wb) & keep, d1 = dppu<DPP_BC + 1>(wb) & keep, d2 = dppu<DPP_BC + 2>(wb) & keep, d3 = dppu<DPP_BC + 3>(wb) & keep;
#pragma unroll
                for (int q = 0; q < 4; ++q) {
                    af[0][q] = hq == q ? d0 : 0u; af[1][q] = hq == q ? d1 : 0u; af[2][q] = hq == q ? d2 : 0u; af[3][q] = hq == q ? d3 : 0u;
                }
                PIN(af[0]); PIN(af[1]); PIN(af[2]); PIN(af[3]);
            }
            BAR();
            STAMP(6);
            v4u o[4];
            unsigned lu;
            for (unsigned s = 0; s < SPIN_CAP; ++s) {
                CBAR();
#pragma unroll
                for (int j = 0; j < 4; ++j) o[j] = ld_c(ws, oo + j * 512);
                lu = ld_c1(ws, SL_OFF + so);
                if (__all(umax(umax(umax(mx4(o[0]), mx4(o[1])), umax(mx4(o[2]), mx4(o[3]))), lu) != SENT)) break;
            }
            STAMP(7);
            f32x4 d = Z4;
#pragma unroll
            for (int j = 0; j < 4; ++j) d = mfma32(af[j], o[j], d);
            const float Ls = add16(rowsum(bflo(wb) * asf(lu)));
            d *= __builtin_amdgcn_rcpf(Ls);
            const unsigned h0 = pk2(d[0], d[1]), h1 = pk2(d[2], d[3]);
            const unsigned l0 = pk2(d[0] - bflo(h0), d[1] - bfhi(h0)), l1 = pk2(d[2] - bflo(h1), d[3] - bfhi(h1));
            *(LAS v2u*)(L + clo) = v2u{h0, h1};
            *(LAS v2u*)(L + clo + 272) = v2u{l0, l1};
            BAR();
        } else {
            int pf[5];
#pragma unroll
            for (int i = 0; i < 4; ++i) {
                const int hh = 16 * hg + 4 * (w & 3) + i;
                pf[i] = OP_OFF + ((b * 32 + hh) * 4 + g) * 8192 + ((kb + 4 * hh + g) & 31) * 256 + i16 * 16;
                PIN(pf[i]);
            }
            {
                const int pc_ = CX_OFF + dh * 2048 + rg * 512 + dcq * 128 + c8 * 16;
                const int ps_ = SM_OFF + (lane >> 2) * 8192 + ((b * 32 + kb) * 32 + 16 * hg) * 4 + (lane & 3) * 16;
                pf[4] = lane < (w == 4 ? 32 : (w == 5 ? 8 : 0)) ? (w == 4 ? pc_ : ps_) : OOB;
                PIN(pf[4]);
            }
            SB();
            const void* wuvp = a.wuv;
            OPQ(wsp); OPQ(wuvp);
            const rsrc_t ws = mk(wsp, WS_BYTES);
#pragma unroll
            for (int i = 0; i < 5; ++i) st_c(ws, pf[i], SENT4);
            want = load_epoch(wsp);
            v4u fr[32];
            LAS unsigned char* tb = L + L_CK + (8 * g + tq) * 1024 + (w & 3) * 256 + (tp & 1) * 8;
            const int k0 = ((g & 1) << 3) | ((tq >> 1) << 2) | ((tq & 1) << 1), k1 = k0 ^ 9;
#define OFR(sg) _Pragma("unroll") for (int tau = 0; tau < 8; ++tau) \
                fr[tau * 4 + sg] = trd(tb + (((2 * tau + (tp >> 1)) ^ k0) * 16) + sg * 32768, tb + (((2 * tau + (tp >> 1)) ^ k1) * 16) + 4096 + sg * 32768);
            BAR(); OFR(0) BAR(); OFR(1) BAR(); OFR(2) fl = ld_c(ws, fo); BAR(); OFR(3)
            int ob[4];
#pragma unroll
            for (int rr = 0; rr < 4; ++rr) {
                const int hh = 16 * hg + 4 * g + rr;
                ob[rr] = OP_OFF + ((b * 32 + hh) * 4 + (w & 3)) * 8192 + ((kb + 4 * hh + (w & 3)) & 31) * 256 + i16 * 16;
                PIN(ob[rr]);
            }
            QGATHER();
            if (w == 7 && lane == 0) st_c2(ws, myfl, want);
            dma_loop(mks(wuvp, 512 * 4096 * 2), 8, rg * 8192 - (i16 & 8) * 16, i16, 0x66442200ull,
                     (128 * dcq + 64 * (w & 1)) * 8192 + dh * 256 + ((w >> 1) & 1) * 128, 8 * 8192, L_WV + (w & 3) * 8192);
            BAR();
            const f32x4 m4 = *(LAS f32x4*)(L + L_MX + i16 * 16);
            f32x4 o[8] = {Z4, Z4, Z4, Z4, Z4, Z4, Z4, Z4};
            f32x4 sv[8];
            v4u pp[4];
            LAS unsigned char* sp = L + L_S + i16 * SPITCH + g * 32;
#pragma unroll
            for (int i = 0; i < 8; ++i) sv[i] = *(LAS f32x4*)(sp + (i >> 1) * 128 + (i & 1) * 16);
            const float m = fmaxf(fmaxf(m4[0], m4[1]), fmaxf(m4[2], m4[3]));
#define PEXP(sg, q) pp[sg][q] = pk2(__builtin_amdgcn_exp2f(sv[2 * (sg) + ((q) >> 1)][2 * ((q) & 1)] - m), __builtin_amdgcn_exp2f(sv[2 * (sg) + ((q) >> 1)][2 * ((q) & 1) + 1] - m))
            PEXP(0, 0); PEXP(0, 1); PEXP(0, 2); PEXP(0, 3);
            SB();
#pragma unroll
            for (int sg = 0; sg < 4; ++sg) {
#pragma unroll
                for (int t2 = 0; t2 < 4; ++t2) {
                    o[2 * t2] = mfma32(pp[sg], fr[(2 * t2) * 4 + sg], o[2 * t2]);
                    o[2 * t2 + 1] = mfma32(pp[sg], fr[(2 * t2 + 1) * 4 + sg], o[2 * t2 + 1]);
                    if (sg < 3) PEXP(sg + 1, t2);
                    SB();
                }
            }
#pragma unroll
            for (int rr = 0; rr < 4; ++rr)
                st_c(ws, ob[rr], v4u{san(pk2(o[0][rr], o[1][rr])), san(pk2(o[2][rr], o[3][rr])), san(pk2(o[4][rr], o[5][rr])), san(pk2(o[6][rr], o[7][rr]))});
            if (w == 7) {
                f32x4 l = Z4;
#pragma unroll
                for (int sg = 0; sg < 4; ++sg) l = mfma32(ONES, pp[sg], l);
                st_c1(ws, lane < 16 ? SL_OFF + ((b * 32 + kb) * 32 + 16 * hg + i16) * 4 : OOB, asu(l[0]));
            }
            STAMP(5);
            BAR();
            VMW(4);
            BAR();
            v4u vf[16];
            {
                LAS unsigned char* vb = L + L_WV + (w & 1) * 16384 + (16 * tq + g) * 128 + tp * 8;
#pragma unroll
                for (int tau = 0; tau < 4; ++tau)
#pragma unroll
                    for (int s = 0; s < 4; ++s) {
                        LAS unsigned char* p = vb + ((tau ^ tq) * 32) + s * 512;
                        vf[tau * 4 + s] = trd(p, p + 8192);
                    }
            }
            int co = CX_OFF + dh * 2048 + (2 * (w & 1) + (g & 1)) * 512 + (2 * dcq + (g >> 1)) * 64 + i16 * 4;
            PIN(co);
            BAR();
            if (w < 6) {
                v4u af[4];
#pragma unroll
                for (int s = 0; s < 4; ++s) af[s] = *(LAS v4u*)(L + L_CL + i16 * 272 + (32 * s + 8 * g) * 2);
                f32x4 d[4] = {Z4, Z4, Z4, Z4};
#pragma unroll
                for (int s = 0; s < 4; ++s)
#pragma unroll
                    for (int tau = 0; tau < 4; ++tau) d[tau] = mfma32(af[s], vf[tau * 4 + s], d[tau]);
                const float e0 = rs16(sum4(d[0]), sum4(d[2])), e1 = rs16(sum4(d[1]), sum4(d[3]));
                st_c1(ws, co, san(pk2(e0, e1)));
            }
        }
        STAMP(8);
        DUMP();
        return;
    }

    const int ai = r - 16, h = dh, part = dcq;
    const bool isF = ai < 9;
    const int fu = 9 * xg + ai;
    const int e = ai - 9, kg = xg >> 1, et = (xg & 1) ? 5 + e : e;
    const bool isE = !isF && (e < ((xg & 1) ? 4 : 5));
    unsigned char* wsp = a.ws; const void* xp = a.x;
    if (w < 2) {
        __builtin_amdgcn_s_setprio(3);
        int xo = (lane >> 5) * 1152 + (lane & 31) * 36;
        const int ah = 4 * xg + ((ai + w) & 3), p8 = 2 * (ai >> 2) + w;
        int xe = ((i16 >> 3) * 576 + 72 * p8 + 18 * g) * 2;
        int qo = (72 * p8 + 18 * g) * 8192 + ah * 256 + i16 * 16;
        int qpo = QP_OFF + ah * 8192 + (i16 >> 2) * 2048 + (g & 1) * 1024 + ((g >> 1) * 8 + p8) * 64 + (i16 & 3) * 16;
        unsigned sl[4], sh[4];
        v4u ones;
#pragma unroll
        for (int q = 0; q < 4; ++q) {
            sl[q] = hq == q ? ((i16 & 1) ? 0x01000c0cu : 0x0c0c0100u) : 0x0c0c0c0cu;
            sh[q] = hq == q ? ((i16 & 1) ? 0x03020c0cu : 0x0c0c0302u) : 0x0c0c0c0cu;
            ones[q] = hq == q ? (hm & 0x3F803F80u) : 0u;
            PIN(sl[q]); PIN(sh[q]);
        }
        PIN(ones);
        PIN(xo); PIN(xe); PIN(qo); PIN(qpo);
        SB();
        const void* wqp = a.wq;
        OPQ(wsp); OPQ(xp); OPQ(wqp);
        const rsrc_t ws = mk(wsp, WS_BYTES);
        const rsrc_t rx = mk(xp, 2 * 576 * 2);
        const rsrc_t rq = mk(wqp, 576 * 4096 * 2);
        want = load_epoch(wsp);
        const v4u x0 = __builtin_amdgcn_raw_buffer_load_b128(rx, xo, 0, 0), x1 = __builtin_amdgcn_raw_buffer_load_b128(rx, xo + 16, 0, 0);
        const unsigned x2 = __builtin_amdgcn_raw_buffer_load_b32(rx, xo + 32, 0, 0);
        const v4u y0 = __builtin_amdgcn_raw_buffer_load_b128(rx, xe, 0, 0), y1 = __builtin_amdgcn_raw_buffer_load_b128(rx, xe + 16, 0, 0);
        const unsigned y2 = __builtin_amdgcn_raw_buffer_load_b32(rx, xe + 32, 0, 0);
        v4u wq[18];
#pragma unroll
        for (int j = 0; j < 18; ++j) wq[j] = __builtin_amdgcn_raw_buffer_load_b128(rq, qo, j * 8192, AUX_NT);
        SB();
        const unsigned xw[9] = {x0.x, x0.y, x0.z, x0.w, x1.x, x1.y, x1.z, x1.w, x2};
        const unsigned yw[9] = {y0.x, y0.y, y0.z, y0.w, y1.x, y1.y, y1.z, y1.w, y2};
        float s = 0.f, s2 = 0.f;
#pragma unroll
        for (int q = 0; q < 9; ++q) { s = dot2(xw[q], 0x3F803F80u, s); s2 = dot2(xw[q], xw[q], s2); }
        float mean = add16(rowsum(s)) * (1.0f / 576.0f);
        float rs = __builtin_amdgcn_rsqf(fmaxf(add16(rowsum(s2)) * (1.0f / 576.0f) - mean * mean, 0.f) + 1e-5f) * (0.08838834764831845f * 1.4426950408889634f);
        PIN(mean); PIN(rs);
        SB();
        STAMP(1);
        f32x4 acc = Z4, cs = Z4;
#pragma unroll
        for (int j = 0; j < 18; ++j) {
            v4u af;
#pragma unroll
            for (int q = 0; q < 4; ++q) af[q] = __builtin_amdgcn_perm(0u, yw[j >> 1], (j & 1) ? sh[q] : sl[q]);
            cs = mfma32(ones, wq[j], cs);
            acc = mfma32(af, wq[j], acc);
        }
        const f32x4 v = (acc - cs * mean) * rs;
        st_c(ws, qpo, v4u{pk2(v[0], v[1]), want.x, pk2(v[2], v[3]), want.x});
        __builtin_amdgcn_s_setprio(0);
        STAMP(2);
    } else {
        const bool slowF = isF && (fu & 7) == 3;
        int bx = xg, bai = ai, wb = w - 4;
        bool doB = (w == 4 || w == 5) && !slowF;
        if (w == 6 && !isF && !isE) {
            if (e >= 5) { bai = (3 - xg) & 7; wb = e - 5; doB = true; }
            else if (xg == 3 || xg == 1) { bx = 3; bai = 8; wb = xg >> 1; doB = true; }
        }
        if (doB) {
            const int h = 4 * bx + ((bai + 2 + wb) & 3), part = bai >> 2;
            int qa = QA_OFF + (((g >> 1) * 32 + h) * 512 + 128 * part + 64 * wb + 4 * i16 + 2 * (g & 1)) * 2;
            PIN(qa);
            SB();
            const void* wukp = a.wuk;
            OPQ(wsp); OPQ(wukp);
            const rsrc_t ws = mk(wsp, WS_BYTES);
            st_c1(ws, qa, SENT);
            want = load_epoch(wsp);
            __builtin_amdgcn_s_sleep(B_DELAY);
            dma_loop(mks(wukp, 512 * 4096 * 2), 16, g * 8192, i16, 0xFEDCBA9876543210ull, (128 * part + 64 * wb) * 8192 + h * 256, 4 * 8192,
                     L_UK + (w - 4) * 16384);
            VMW(16);
            if (lane == 0) st_c2(ws, FLQ_OFF + ((bx * 16 + bai) * 2 + wb) * 8, want);
            int po = QP_OFF + h * 8192 + i16 * 64 + g * 16;
            PIN(qa); PIN(po);
            VMW(1);
            STAMP(4);
            v4u qf[8];
#pragma unroll
            for (int j = 0; j < 8; ++j) qf[j] = ld_c(ws, po + j * 1024);
            v4u uk[16];
#pragma unroll
            for (int tau = 0; tau < 4; ++tau)
#pragma unroll
                for (int s = 0; s < 4; ++s) uk[tau * 4 + s] = *(LAS v4u*)(L + L_UK + (w - 4) * 16384 + i16 * 1024 + tau * 256 + (((4 * s + g) ^ i16) * 16));
            for (unsigned s = 0; s < SPIN_CAP; ++s) {
                unsigned dd = 0;
#pragma unroll
                for (int j = 0; j < 8; ++j) dd |= (qf[j].y ^ want.x) | (qf[j].w ^ want.x);
                if (__all(dd == 0u)) break;
                CBAR();
#pragma unroll
                for (int j = 0; j < 8; ++j) qf[j] = ld_c(ws, po + j * 1024);
            }
            STAMP(5);
            f32x4 d[4] = {Z4, Z4, Z4, Z4};
#pragma unroll
            for (int s = 0; s < 4; ++s) {
                const v4u q = {qf[2 * s].x, qf[2 * s].z, qf[2 * s + 1].x, qf[2 * s + 1].z};
#pragma unroll
                for (int tau = 0; tau < 4; ++tau) d[tau] = mfma32(q, uk[tau * 4 + s], d[tau]);
            }
            const float e0 = rs16(sum4(d[0]), sum4(d[2])), e1 = rs16(sum4(d[1]), sum4(d[3]));
            st_c1(ws, qa, san(pk2(e0, e1)));
            STAMP(6);
        } else {
            const int w3 = w < 4 ? w - 2 : (w < 6 ? 3 : w - 4);
            const int pf_ = FO_OFF + fu * 4608 + (w3 * 128 + lane) * 16;
            const int pe = AO_OFF + (2 * kg + rg) * 1152 + et * 128 + c8 * 16;
            const int lim = isF ? 288 - w3 * 128 : ((isE && w3 == 0) ? 16 : 0);
            int p0 = lane < lim ? (isF ? pf_ : pe) : OOB, p1 = lane + 64 < lim ? pf_ + 1024 : OOB;
            PIN(p0); PIN(p1);
            SB();
            OPQ(wsp);
            const rsrc_t ws = mk(wsp, WS_BYTES);
            st_c(ws, p0, SENT4);
            st_c(ws, p1, SENT4);
            want = load_epoch(wsp);
            VMW(0);
        }
    }
    OPQ(want);
    const rsrc_t ws = mk(wsp, WS_BYTES);
    const rsrc_t rx = mk(xp, 2 * 576 * 2);
    BAR();
    if (w == 7 && lane == 0) st_c2(ws, myfl, want);
    __builtin_amdgcn_s_sleep(1);

    if (isF) {
        if ((fu & 7) != 3) for (int z_ = 0; z_ < F_DELAY; ++z_) __builtin_amdgcn_s_sleep(8);
        {
            const int key = ((rg >> 1) & 1) | ((w & 1) << 1);
            dma_loop(mks(a.wd, 4608 * 576 * 2), 9, rg * 1152 - (i16 & 8) * 16, i16 ^ (key << 1), 0ull, (64 * fu + 8 * w) * 1152, 128, L_DN + w * 1024, 8192);
        }
        const int keyg = (((w & 3) >> 1) & 1) | (((rg >> 2) & 1) << 1);
        const v4u sgu = mks((w >> 2) ? a.wu : a.wg, 576 * 4608 * 2);
        const int sgo = 8 * (w & 3) * 9216 + fu * 128;
        dma_loop(sgu, 10, rg * 9216 - (i16 & 8) * 16, i16 ^ (keyg << 1), 0ull, sgo, 32 * 9216, L_GU1 + w * 1024, 8192);
        VMW(10);
        BAR();
        v4u df[10];
        {
            const int key = ((tq >> 1) & 1) | ((g & 1) << 1);
            LAS unsigned char* tb = L + L_DN + (w >> 2) * 8192 + (8 * g + tq) * 128 + tp * 8 + (((w & 3) ^ key) * 32);
#pragma unroll
            for (int i = 0; i < 5; ++i)
#pragma unroll
                for (int s = 0; s < 2; ++s) {
                    LAS unsigned char* p = tb + (i < 4 ? i : (w < 4 ? 4 : 0)) * 16384 + s * 4096;
                    df[i * 2 + s] = trd(p, p + 512);
                }
        }
        BAR();
        dma_loop(sgu, 8, rg * 9216 - (i16 & 8) * 16, i16 ^ (keyg << 1), 0ull, sgo + 10 * 32 * 9216, 32 * 9216, L_GU2 + w * 1024, 8192);
        VMW(8);
        BAR();
        v4u gf[18], uf[18];
        const int keyp = (tq & 1) | ((g & 1) << 1);
        const int tro = (16 * (tq & 1) + 4 * g + (tq >> 1)) * 128 + tp * 8 + (((w & 3) ^ keyp) * 32);
        if (w < 4) {
#pragma unroll
            for (int s = 0; s < 10; ++s) {
                LAS unsigned char* p = L + L_GU1 + s * 8192 + tro;
                gf[s] = trd(p, p + 256);
                uf[s] = trd(p + 4096, p + 4096 + 256);
            }
        }
        BAR();
        if (w >= 4) {
            const int t = tid - 256;
#pragma unroll
            for (int i = 0; i < 3; ++i) {
                const int k = t + 256 * i;
                if (k < 576) {
                    const int bb = k >= 288, kk = k - 288 * bb, m0 = bb * 576 + (kk >> 4) * 32 + (kk & 15);
                    const unsigned lo = __builtin_amdgcn_raw_buffer_load_b16(rx, m0 * 2, 0, 0), hi = __builtin_amdgcn_raw_buffer_load_b16(rx, (m0 + 16) * 2, 0, 0);
                    *(LAS unsigned*)(L + L_AO + (8 + bb) * AOP + kk * 4) = lo | (hi << 16);
                }
            }
#pragma unroll
            for (int i = 0; i < 2; ++i) {
                const int k = t + 256 * i;
                if (k < 6 * 73) *(LAS v4u*)(L + L_AO + 10 * AOP + k * 16) = v4u{0u, 0u, 0u, 0u};
            }
            if (w == 6) check_flags(ws, want, lane);
        }
        VMW(0);
        BAR();
        STAMP(7);
        float csg = 0.f, csu = 0.f;
        if (w < 4) {
#pragma unroll
            for (int s = 10; s < 18; ++s) {
                LAS unsigned char* p = L + L_GU2 + (s - 10) * 8192 + tro;
                gf[s] = trd(p, p + 256);
                uf[s] = trd(p + 4096, p + 4096 + 256);
            }
            f32x4 cg = Z4, cu = Z4;
#pragma unroll
            for (int s = 0; s < 18; ++s) { cg = mfma32(ONES, gf[s], cg); cu = mfma32(ONES, uf[s], cu); }
            csg = cg[0]; csu = cu[0];
            PIN(csg); PIN(csu);
        } else {
            const int wq_ = w - 4;
            int go[3], lo_[3];
#pragma unroll
            for (int i = 0; i < 3; ++i) {
                const int id = i * 64 + lane;
                go[i] = id < 144 ? AO_OFF + wq_ * 2304 + id * 16 : OOB;
                lo_[i] = id < 144 ? L_AO + wq_ * 2 * AOP + id * 16 + (id >= 72 ? 16 : 0) : L_JUNK + lane * 16;
                PIN(lo_[i]);
            }
            v4u av[3];
            for (unsigned s = 0; s < SPIN_CAP; ++s) {
                CBAR();
#pragma unroll
                for (int i = 0; i < 3; ++i) av[i] = ld_c(ws, go[i]);
                if (__all(umax(umax(mx4(av[0]), mx4(av[1])), mx4(av[2])) != SENT)) break;
            }
            STAMP(8);
#pragma unroll
            for (int i = 0; i < 3; ++i) *(LAS v4u*)(L + lo_[i]) = av[i];
        }
        int fo = i16 < 2 ? FO_OFF + fu * 4608 + w * 128 + i16 * 64 + g * 16 : OOB;
        PIN(fo);
        BAR();
        LAS unsigned char* ab = L + L_AO + i16 * AOP + g * 16;
        f32x4 dg = Z4, du = Z4;
        if (w < 4) {
            v4u af[6];
#pragma unroll
            for (int s = 0; s < 6; ++s) af[s] = *(LAS v4u*)(ab + s * 64);
            SB();
#pragma unroll
            for (int s = 0; s < 18; ++s) {
                const v4u cur = af[s % 6];
                dg = mfma32(cur, gf[s], dg);
                du = mfma32(cur, uf[s], du);
                if (s + 6 < 18) af[s % 6] = *(LAS v4u*)(ab + (s + 6) * 64);
                SB();
            }
        } else {
            const int wq_ = w - 4;
            v4u af[5];
#pragma unroll
            for (int i = 0; i < 5; ++i) af[i] = *(LAS v4u*)(ab + (i < 4 || wq_ < 2 ? wq_ + 4 * i : 0) * 64);
#pragma unroll
            for (int i = 0; i < 5; ++i) {
                if (i < 4 || wq_ < 2) {
                    dg = mfma32(af[i], af[i], dg);
                    du = mfma32(af[i], ONES, du);
                }
            }
            float s2 = (i16 & 1) ? dg[1] + dg[3] : dg[0] + dg[2];
            const float s1 = (i16 & 1) ? du[1] + du[3] : du[0] + du[2];
            s2 += dpp<DPP_ROR8>(s2); s2 += dpp<DPP_ROR4>(s2); s2 += dpp<DPP_ROR2>(s2);
            const float st = add32(rs16(s1, s2));
            if (lane < 32 && i16 < 2) *(LAS float*)(L + L_FS + ((i16 * 2 + g) * 4 + wq_) * 4) = st;
        }
        BAR();
        if (w < 4) {
            const f32x4 s1 = *(LAS f32x4*)(L + L_FS + (g >> 1) * 32), s2 = *(LAS f32x4*)(L + L_FS + (g >> 1) * 32 + 16);
            const float gs = add16(rs32(dg[0] + dg[2], dg[1] + dg[3])), us = add16(rs32(du[0] + du[2], du[1] + du[3]));
            const float mean = sum4(s1) * (1.0f / 576.0f);
            const float var = fmaxf(sum4(s2) * (1.0f / 576.0f) - mean * mean, 0.f);
            const float rstd = __builtin_amdgcn_rsqf(var + 1e-5f);
            const float gg = (gs - mean * csg) * rstd, uu = (us - mean * csu) * rstd;
            const float act = gg * uu * __builtin_amdgcn_rcpf(1.0f + __builtin_amdgcn_exp2f(gg * -1.4426950408889634f));
            if ((g & 1) == 0) *(LAS unsigned short*)(L + L_ACT + (g >> 1) * 128 + (16 * w + i16) * 2) = (unsigned short)pk2(act, act);
        }
        BAR();
        {
            const v4u b0 = *(LAS v4u*)(L + L_ACT + (i16 & 1) * 128 + g * 16), b1 = *(LAS v4u*)(L + L_ACT + (i16 & 1) * 128 + 64 + g * 16);
#pragma unroll
            for (int i = 0; i < 5; ++i) {
                if (i < 4 || w < 4) {
                    f32x4 d = mfma32(df[2 * i + 1], b1, mfma32(df[2 * i], b0, Z4));
                    st_c(ws, fo + i * 1024, __builtin_bit_cast(v4u, d));
                }
            }
        }
        STAMP(9);
        DUMP();
        return;
    }
    if (!isE) {
        if (blockIdx.x == 255 && w == 3) {
            check_flags(ws, want, lane);
            if (lane == 0) st_c2(ws, EPOCH_OFF, want);
        }
        DUMP();
        return;
    }
    {
        const int hh = 8 * kg + w;
        for (int z_ = 0; z_ < E_DELAY; ++z_) __builtin_amdgcn_s_sleep(8);
        dma_loop(mks(a.wo, 4096 * 576 * 2), 16, rg * 1152 - (i16 & 8) * 16, i16 ^ (((rg >> 2) & 1) << 2), 0x2200220022002200ull,
                 hh * 128 * 1152 + et * 128, 8 * 1152, L_WO + w * 16384);
        if (w == 6) check_flags(ws, want, lane);
        VMW(0);
        v4u wf[16];
        {
            const int keyp = (tq & 1) | ((g & 1) << 1);
            LAS unsigned char* tb = L + L_WO + w * 16384 + (16 * (tq & 1) + 4 * g + (tq >> 1)) * 128 + tp * 8;
#pragma unroll
            for (int tau = 0; tau < 4; ++tau)
#pragma unroll
                for (int s = 0; s < 4; ++s) {
                    LAS unsigned char* p = tb + ((tau ^ keyp) * 32) + s * 4096;
                    wf[tau * 4 + s] = trd(p, p + 256);
                }
        }
        int ao = AO_OFF + (2 * kg + (lane >> 5)) * 1152 + et * 128 + (lane & 31) * 4;
        PIN(ao);
        BAR();
        STAMP(7);
        v4u cf[4];
        const int co = i16 < 8 ? CX_OFF + hh * 2048 + i16 * 64 + g * 16 : OOB;
        for (unsigned s = 0; s < SPIN_CAP; ++s) {
            CBAR();
#pragma unroll
            for (int j = 0; j < 4; ++j) cf[j] = ld_c(ws, co + j * 512);
            if (__all(umax(umax(mx4(cf[0]), mx4(cf[1])), umax(mx4(cf[2]), mx4(cf[3]))) != SENT)) break;
        }
        STAMP(8);
        {
            f32x4 d[4] = {Z4, Z4, Z4, Z4};
#pragma unroll
            for (int s = 0; s < 4; ++s)
#pragma unroll
                for (int tau = 0; tau < 4; ++tau) d[tau] = mfma32(cf[s], wf[tau * 4 + s], d[tau]);
            const f32x4 ev = {rs16(d[0][0] + d[0][2], d[2][0] + d[2][2]), rs16(d[1][0] + d[1][2], d[3][0] + d[3][2]),
                              rs16(d[0][1] + d[0][3], d[2][1] + d[2][3]), rs16(d[1][1] + d[1][3], d[3][1] + d[3][3])};
            if (lane < 32) *(LAS f32x4*)(L + L_RE + w * 512 + lane * 16) = ev;
        }
        BAR();
        const int ru = 9 * kg + et;
        if (w == 0) {
            LAS unsigned char* p = L + L_RE + (lane >> 5) * 2048 + (lane & 31) * 16;
            const f32x4 v = (*(LAS f32x4*)p + *(LAS f32x4*)(p + 512)) + (*(LAS f32x4*)(p + 1024) + *(LAS f32x4*)(p + 1536));
            st_c1(ws, ao, san(pk2(rs32(v[0], v[2]), rs32(v[1], v[3]))));
            STAMP(9);
        } else if (w == 1) {
            const v2u xr = __builtin_amdgcn_raw_buffer_load_b64(rx, rg == 4 ? ((c8 >> 2) * 576 + 16 * ru + 4 * (c8 & 3)) * 2 : OOB, 0, 0);
            __builtin_amdgcn_s_sleep(R_DELAY);
            v4u hv;
            const int ho = rg < 4 ? AO_OFF + (2 * rg + (c8 >> 2)) * 1152 + (ru >> 1) * 64 + (c8 & 3) * 16 : OOB;
            for (unsigned s = 0; s < SPIN_CAP; ++s) {
                CBAR();
                hv = ld_c(ws, ho);
                if (__all(mx4(hv) != SENT)) break;
            }
            f32x4 acc = {bflo(xr.x), bfhi(xr.x), bflo(xr.y), bfhi(xr.y)};
#pragma unroll
            for (int j = 0; j < 4; ++j) acc[j] += (ru & 1) ? bfhi(hv[j]) : bflo(hv[j]);
            PIN(acc);
            v4u fv[9];
            const int fo = FO_OFF + rg * 4608 + ru * 128 + c8 * 16;
            for (unsigned s = 0; s < SPIN_CAP; ++s) {
                CBAR();
#pragma unroll
                for (int j = 0; j < 9; ++j) fv[j] = ld_c(ws, fo + j * 8 * 4608);
                unsigned m = mx4(fv[0]);
#pragma unroll
                for (int j = 1; j < 9; ++j) m = umax(m, mx4(fv[j]));
                if (__all(m != SENT)) break;
            }
            STAMP(10);
#pragma unroll
            for (int j = 0; j < 9; ++j) acc += __builtin_bit_cast(f32x4, fv[j]);
            float u0 = rs32(acc[0], acc[2]), u1 = rs32(acc[1], acc[3]);
            u0 = add16(u0); u1 = add16(u1);
            u0 += dpp<DPP_ROR8>(u0); u1 += dpp<DPP_ROR8>(u1);
            if ((lane & 31) < 8)
                *(unsigned*)((unsigned char*)a.out + ((c8 >> 2) * 576 + 16 * ru + 4 * (c8 & 3) + 2 * (lane >> 5)) * 2) = pk2(u0, u1);
            STAMP(11);
        }
        DUMP();
    }
}

}

extern "C" void kernel_launch(void* const* d_in, const int* in_sizes, int n_in, void* d_out, int out_size, void* d_ws,
                              size_t ws_size, hipStream_t stream) {
    Args a;
    a.x = d_in[0]; a.ckv = d_in[1]; a.wq = d_in[2]; a.wuk = d_in[3]; a.wuv = d_in[4]; a.wo = d_in[5];
    a.wg = d_in[7]; a.wu = d_in[8]; a.wd = d_in[9];
    a.out = d_out; a.ws = (unsigned char*)d_ws;
    fprintf(stderr, "PTRS x %p ckv %p wq %p wuk %p wuv %p wo %p wg %p wu %p wd %p out %p ws %p\n", a.x, a.ckv, a.wq, a.wuk, a.wuv, a.wo, a.wg, a.wu, a.wd, a.out, a.ws);
    (void)hipFuncSetAttribute((const void*)fused, hipFuncAttributeMaxDynamicSharedMemorySize, LDS_BYTES);
    hipLaunchKernelGGL(fused, dim3(256), dim3(NT), LDS_BYTES, stream, a);
}
```

```cpp
#include <hip/hip_runtime.h>
#include <hip/hip_bf16.h>

#define LAS __attribute__((address_space(3)))
typedef unsigned v4u __attribute__((ext_vector_type(4)));
typedef unsigned v2u __attribute__((ext_vector_type(2)));
typedef float f32x4 __attribute__((ext_vector_type(4)));
typedef float f32x2 __attribute__((ext_vector_type(2)));
typedef short s16x4 __attribute__((ext_vector_type(4)));
typedef __bf16 bf16x8 __attribute__((ext_vector_type(8)));
typedef __bf16 bf16x2 __attribute__((ext_vector_type(2)));
typedef __amdgpu_buffer_rsrc_t rsrc_t;
#define DI __device__ __forceinline__

#define STAMPS 0

#pragma clang diagnostic ignored "-Winline-asm"
#define KVER 10
namespace {

constexpr int NT = 512, NW = 8;
constexpr unsigned SENT = 0xFFFFFFFFu;
constexpr int AUX_NT = 2, AUX_SC1 = 16;
constexpr unsigned SPIN_CAP = 1u << 18;
constexpr int OOB = 0x7ffff000;

constexpr int EPOCH_OFF = 0;
constexpr int FL_OFF = 1024;
constexpr int FLQ_OFF = 4096;
constexpr int QP_OFF = 8192;
constexpr int QA_OFF = QP_OFF + 262144;
constexpr int SM_OFF = QA_OFF + 65536;
constexpr int SL_OFF = SM_OFF + 8192;
constexpr int CX_OFF = SL_OFF + 8192;
constexpr int AO_OFF = CX_OFF + 65536;
constexpr int FO_OFF = AO_OFF + 9216;
constexpr int OP_OFF = 1 << 20;
constexpr int WS_BYTES = OP_OFF + (2 << 20);
constexpr int DBG_OFF = 8 << 20;

constexpr int L_CK = 0;
constexpr int L_Q = 131072;
constexpr int SPITCH = 528;
constexpr int L_S = L_Q + 16384;
constexpr int L_MX = L_S + 16 * SPITCH;
constexpr int L_WV = 0;
constexpr int L_CL = L_Q;
constexpr int L_UK = 0;
constexpr int L_DN = 0;
constexpr int L_GU1 = 73728;
constexpr int L_GU2 = 0;
constexpr int L_AO = 73728;
constexpr int AOP = 1168;
constexpr int L_FS = 155648;
constexpr int L_ACT = L_FS + 64;
constexpr int L_WO = 0;
constexpr int L_RE = 131072;
constexpr int L_DBG = 156672;
constexpr int L_JUNK = L_DBG + 1024;
constexpr int LDS_BYTES = L_JUNK + 1024;
static_assert(L_MX + 256 <= L_DBG && L_ACT + 256 <= L_DBG, "lds map");
__host__ __device__ constexpr int ckey(int row) { return ((((row >> 3) ^ (row >> 2)) & 1) << 3) | (((row >> 1) & 1) << 2) | ((row & 1) << 1) | ((row >> 2) & 1); }
constexpr unsigned long long ckey_tab() { unsigned long long t = 0; for (int i = 0; i < 16; ++i) t |= (unsigned long long)ckey(i) << (4 * i); return t; }

DI v4u ld_nt(rsrc_t r, int off) { return __builtin_amdgcn_raw_buffer_load_b128(r, off, 0, AUX_NT); }
DI v4u ld_c(rsrc_t r, int off) { return __builtin_amdgcn_raw_buffer_load_b128(r, off, 0, AUX_SC1); }
DI v2u ld_c2(rsrc_t r, int off) { return __builtin_amdgcn_raw_buffer_load_b64(r, off, 0, AUX_SC1); }
DI unsigned ld_c1(rsrc_t r, int off) { return __builtin_amdgcn_raw_buffer_load_b32(r, off, 0, AUX_SC1); }
DI void st_c(rsrc_t r, int off, v4u v) { __builtin_amdgcn_raw_buffer_store_b128(v, r, off, 0, AUX_SC1); }
DI void st_c2(rsrc_t r, int off, v2u v) { __builtin_amdgcn_raw_buffer_store_b64(v, r, off, 0, AUX_SC1); }
DI void st_c1(rsrc_t r, int off, unsigned v) { __builtin_amdgcn_raw_buffer_store_b32(v, r, off, 0, AUX_SC1); }
DI unsigned san(unsigned x) { return x < 0xFFFFFFFEu ? x : 0xFFFFFFFEu; }
DI rsrc_t mk(const void* p, unsigned bytes) { return __builtin_amdgcn_make_buffer_rsrc((void*)p, 0, bytes, 0x00020000); }
DI v4u mks(const void* p, unsigned bytes) {
    const unsigned long long a = (unsigned long long)p;
    return v4u{(unsigned)a, (unsigned)(a >> 32) & 0xFFFFu, bytes, 0x00020000u};
}
DI void dma(v4u srd, int voff, int soff, int lds) {
    asm volatile("s_mov_b32 m0, %0\n\ts_nop 0\n\tbuffer_load_dwordx4 %1, %2, %3 offen nt lds" ::"s"(lds), "v"(voff), "s"(srd), "s"(soff) : "m0", "memory");
}
DI void dmap(v4u srd, int voff, int soff, int lds) {
    asm volatile("s_mov_b32 m0, %0\n\ts_nop 0\n\tbuffer_load_dwordx4 %1, %2, %3 offen lds" ::"s"(lds), "v"(voff), "s"(srd), "s"(soff) : "m0", "memory");
}
DI float asf(unsigned u) { return __builtin_bit_cast(float, u); }
DI unsigned asu(float f) { return __builtin_bit_cast(unsigned, f); }
DI float bflo(unsigned u) { return asf(u << 16); }
DI float bfhi(unsigned u) { return asf(u & 0xFFFF0000u); }
DI unsigned pk2(float a, float b) { bf16x2 t = {(__bf16)a, (__bf16)b}; return __builtin_bit_cast(unsigned, t); }
DI float dot2(unsigned a, unsigned b, float c) { return __builtin_amdgcn_fdot2_f32_bf16(__builtin_bit_cast(bf16x2, a), __builtin_bit_cast(bf16x2, b), c, false); }
DI f32x4 mfma32(v4u a, v4u b, f32x4 c) {
    return __builtin_amdgcn_mfma_f32_16x16x32_bf16(__builtin_bit_cast(bf16x8, a), __builtin_bit_cast(bf16x8, b), c, 0, 0, 0);
}
template <int CTRL> DI float dpp(float v) { return asf((unsigned)__builtin_amdgcn_update_dpp(0, (int)asu(v), CTRL, 0xf, 0xf, true)); }
template <int CTRL> DI unsigned dppu(unsigned v) { return (unsigned)__builtin_amdgcn_update_dpp(0, (int)v, CTRL, 0xf, 0xf, true); }
constexpr int DPP_X1 = 0xB1, DPP_X2 = 0x4E, DPP_HMIR = 0x141, DPP_ROR8 = 0x128, DPP_ROR4 = 0x124, DPP_ROR2 = 0x122, DPP_ROR1 = 0x121;
constexpr int DPP_BC = 0x150;
DI float rs16(float a, float b) { auto r = __builtin_amdgcn_permlane16_swap(asu(a), asu(b), false, false); unsigned x = r[0], y = r[1]; return asf(x) + asf(y); }
DI float rs32(float a, float b) { auto r = __builtin_amdgcn_permlane32_swap(asu(a), asu(b), false, false); unsigned x = r[0], y = r[1]; return asf(x) + asf(y); }
DI float add16(float v) { return rs16(v, v); }
DI float add32(float v) { return rs32(v, v); }
DI float max16(float v) { auto r = __builtin_amdgcn_permlane16_swap(asu(v), asu(v), false, false); unsigned a = r[0], b = r[1]; return fmaxf(asf(a), asf(b)); }
DI float max32(float v) { auto r = __builtin_amdgcn_permlane32_swap(asu(v), asu(v), false, false); unsigned a = r[0], b = r[1]; return fmaxf(asf(a), asf(b)); }
DI float rowsum(float v) { v += dpp<DPP_ROR8>(v); v += dpp<DPP_ROR4>(v); v += dpp<DPP_ROR2>(v); v += dpp<DPP_ROR1>(v); return v; }
DI float rowmax(float v) { v = fmaxf(v, dpp<DPP_ROR8>(v)); v = fmaxf(v, dpp<DPP_ROR4>(v)); v = fmaxf(v, dpp<DPP_ROR2>(v)); v = fmaxf(v, dpp<DPP_ROR1>(v)); return v; }
DI float sum4(f32x4 d) { return (d[0] + d[2]) + (d[1] + d[3]); }
DI unsigned umax(unsigned a, unsigned b) { return a > b ? a : b; }
DI unsigned mx4(v4u v) { return umax(umax(v.x, v.y), umax(v.z, v.w)); }
DI v4u trd(LAS unsigned char* p0, LAS unsigned char* p1) {
    v2u a = __builtin_bit_cast(v2u, __builtin_amdgcn_ds_read_tr16_b64_v4i16((LAS s16x4*)p0));
    v2u b = __builtin_bit_cast(v2u, __builtin_amdgcn_ds_read_tr16_b64_v4i16((LAS s16x4*)p1));
    return v4u{a.x, a.y, b.x, b.y};
}
#define CBAR() asm volatile("" ::: "memory")
#define VMW(n) asm volatile("s_waitcnt vmcnt(" #n ")" ::: "memory")
#define BAR() asm volatile("s_waitcnt lgkmcnt(0)\n\ts_barrier" ::: "memory")
#define PIN(v) asm volatile("" : "+v"(v))
#define SB() __builtin_amdgcn_sched_barrier(0)
#define OPQ(p) asm volatile("" : "+s"(p))
constexpr f32x4 Z4 = {0.f, 0.f, 0.f, 0.f};

DI void check_flags(rsrc_t ws, v2u want, int lane) {
    for (unsigned s = 0; s < SPIN_CAP; ++s) {
        CBAR();
        v4u a = ld_c(ws, FL_OFF + lane * 16), b = ld_c(ws, FL_OFF + 1024 + lane * 16);
        unsigned d = (a.x ^ want.x) | (a.y ^ want.y) | (a.z ^ want.x) | (a.w ^ want.y) | (b.x ^ want.x) | (b.y ^ want.y) | (b.z ^ want.x) | (b.w ^ want.y);
        if (__all(d == 0u)) break;
    }
    CBAR();
}
DI v2u load_epoch(const unsigned char* ws) {
    const unsigned long long e = *(const __attribute__((address_space(4))) unsigned long long*)(ws + EPOCH_OFF) + 1ull;
    return v2u{(unsigned)e, (unsigned)(e >> 32)};
}

#define STAMP(i) do {} while (0)
#define DUMP() do {} while (0)
#define STAMP_INIT() do {} while (0)

struct Args {
    const void *x, *ckv, *wq, *wuk, *wuv, *wo, *wg, *wu, *wd;
    void* out;
    unsigned char* ws;
};
#define G1_DELAY 0
#define B_DELAY 10
#define E_DELAY 6
#define F_DELAY 3
#define WUV_EARLY 0
#define NO_PREFILL 0
#define R_DELAY 20
#define CODE_KB 19

DI void dma_loop(v4u srd, int n, int vhi, int i16, unsigned long long tab, int soff, int sstep, int lds, int lstep = 1024) {
    soff = __builtin_amdgcn_readfirstlane(soff); lds = __builtin_amdgcn_readfirstlane(lds);
    asm volatile("s_nop 4" : "+s"(soff), "+s"(lds));
#pragma unroll 1
    for (int i = 0; i < n; ++i) {
        dma(srd, vhi + ((i16 ^ (int)((unsigned)tab & 15u)) << 4), soff, lds);
        tab >>= 4; soff += sstep; lds += lstep;
    }
}
DI void wait_flags16(rsrc_t ws, int off, v4u fl, v2u want) {
    for (unsigned s = 0; s < SPIN_CAP; ++s) {
        if (__all(((fl.x ^ want.x) | (fl.y ^ want.y) | (fl.z ^ want.x) | (fl.w ^ want.y)) == 0u)) break;
        CBAR();
        fl = ld_c(ws, off);
    }
    CBAR();
}

__global__ void __launch_bounds__(NT, 2) fused(Args a) {
    extern __shared__ __attribute__((aligned(16))) unsigned char smem[];
    LAS unsigned char* L = (LAS unsigned char*)smem;
    const int tid = threadIdx.x, lane = tid & 63, w = __builtin_amdgcn_readfirstlane(tid >> 6);
    const int xg = blockIdx.x & 7, r = blockIdx.x >> 3;
    const int c8 = lane & 7, rg = lane >> 3, i16 = lane & 15, g = lane >> 4;
    const int tq = i16 >> 2, tp = i16 & 3;
    const v4u SENT4 = {SENT, SENT, SENT, SENT};
    const int dh = 4 * xg + ((r & 15) >> 2), dcq = r & 3;
    const unsigned hm = (i16 & 1) ? 0xFFFF0000u : 0x0000FFFFu;
    const int hq = (i16 >> 1) & 3;
    const v4u ONES = {0x3F803F80u, 0x3F803F80u, 0x3F803F80u, 0x3F803F80u};
    if (w == 7 && r < CODE_KB) {
        unsigned long long pc;
        asm volatile("s_getpc_b64 %0" : "=s"(pc));
        pc &= ~0xFFull;
        const v4u sc = v4u{(unsigned)pc, (unsigned)(pc >> 32) & 0xFFFFu, (unsigned)(CODE_KB * 1024), 0x00020000u};
        dmap(sc, lane * 16, r * 1024, L_JUNK);
    }
    STAMP_INIT();
    STAMP(0);
    v2u want;

    const int myfl = FL_OFF + ((r >> 4) * 128 + xg * 16 + (r & 15)) * 8;
    if (r < 16) {
        const int b = xg >> 2, kb = 8 * (xg & 3) + (r >> 1), hg = r & 1;
        unsigned char* wsp = a.ws;
        const int fo = FLQ_OFF + (w * 16 + i16) * 16;
        v4u fl;
#define QGATHER() \
        int ql0 = L_Q + w * 1024 + ((lane ^ ckey(w)) * 16), ql1 = L_Q + (8 + w) * 1024 + ((lane ^ ckey(8 + w)) * 16); \
        int qoff = QA_OFF + (b * 32 + 16 * hg) * 1024 + tid * 16; \
        PIN(ql0); PIN(ql1); PIN(qoff); \
        VMW(0); \
        OPQ(want); \
        STAMP(1); \
        wait_flags16(ws, fo, fl, want); \
        BAR(); \
        STAMP(2); \
        v4u q0, q1; \
        for (unsigned s = 0; s < SPIN_CAP; ++s) { \
            CBAR(); \
            q0 = ld_c(ws, qoff); q1 = ld_c(ws, qoff + 8192); \
            if (__all(umax(mx4(q0), mx4(q1)) != SENT)) break; \
        } \
        STAMP(3); \
        *(LAS v4u*)(L + ql0) = q0; \
        *(LAS v4u*)(L + ql1) = q1; \
        BAR();
        if (w < 4) {
            const int key = ckey(i16);
            v4u fr[32];
            const unsigned long long tab = (w & 1) ? (ckey_tab() >> 32) : ckey_tab();
            const int cso = (b * 4096 + 128 * kb + 8 * w) * 1024;
            int vhi = g * 256;
            PIN(vhi);
            SB();
            const void* ckvp = a.ckv;
            OPQ(wsp); OPQ(ckvp);
            const rsrc_t ws = mk(wsp, WS_BYTES);
            const v4u sck = mks(ckvp, 2 * 4096 * 512 * 2);
#define CKV(sg) dma_loop(sck, 8, vhi, i16, tab, cso + sg * 32768, 1024, L_CK + sg * 32768 + w * 8192)
#define SFR() do { LAS unsigned char* sb = L + L_CK + (32 * w + i16) * 1024; \
                _Pragma("unroll") for (int s = 0; s < 16; ++s) { \
                    LAS unsigned char* p = sb + ((((4 * s + g) & 15) ^ key) * 16) + (s >> 2) * 256; \
                    fr[s] = *(LAS v4u*)p; fr[16 + s] = *(LAS v4u*)(p + 16384); } } while (0)
#define LSTEP0() do { VMW(16); BAR(); CKV(3); } while (0)
#define LSTEP1() do { VMW(16); BAR(); } while (0)
#define LSTEP2() do { VMW(8); BAR(); } while (0)
#define LSTEP3() do { fl = ld_c(ws, fo); VMW(1); BAR(); } while (0)
            __builtin_amdgcn_s_sleep(G1_DELAY);
            CKV(0); CKV(1); CKV(2);
            want = load_epoch(wsp);
            LSTEP0();
            if (w > 0) { LSTEP1(); if (w > 1) { LSTEP2(); if (w > 2) LSTEP3(); } }
            SFR();
            if (w < 1) LSTEP1();
            if (w < 2) LSTEP2();
            if (w < 3) LSTEP3();
            QGATHER();
            f32x4 s0 = Z4, s1 = Z4;
            LAS unsigned char* qb = L + L_Q + i16 * 1024;
            v4u qf[16];
#pragma unroll
            for (int s = 0; s < 16; ++s) qf[s] = *(LAS v4u*)(qb + ((((4 * s + g) & 15) ^ key) * 16) + (s >> 2) * 256);
            SB();
#pragma unroll
            for (int s = 0; s < 16; ++s) {
                s0 = mfma32(fr[s], qf[s], s0);
                s1 = mfma32(fr[16 + s], qf[s], s1);
            }
            LAS unsigned char* sp = L + L_S + i16 * SPITCH + (32 * w + 4 * g) * 4;
            *(LAS f32x4*)sp = s0;
            *(LAS f32x4*)(sp + 64) = s1;
            float mx = fmaxf(fmaxf(fmaxf(s0[0], s0[1]), fmaxf(s0[2], s0[3])), fmaxf(fmaxf(s1[0], s1[1]), fmaxf(s1[2], s1[3])));
            mx = max32(max16(mx));
            if (lane < 16) *(LAS float*)(L + L_MX + (i16 * 4 + w) * 4) = mx;
            STAMP(4);
            BAR();
            if (w == 0) {
                const f32x4 m4 = *(LAS f32x4*)(L + L_MX + i16 * 16);
                const float m = fmaxf(fmaxf(m4[0], m4[1]), fmaxf(m4[2], m4[3]));
                st_c1(ws, lane < 16 ? SM_OFF + ((b * 32 + kb) * 32 + 16 * hg + i16) * 4 : OOB, asu(m));
            }
            const int kbl = (2 * ((i16 + 4 * w) & 15) + (g & 1) - 4 * dh - dcq) & 31;
            int so = ((g >> 1) * 32 + kbl) * 128 + dh * 4;
            int oo = OP_OFF + (((g >> 1) * 32 + dh) * 4 + dcq) * 8192 + (8 * w + (g & 1)) * 256 + i16 * 16;
            int clo = L_CL + ((g >> 1) * 8 + 2 * w) * 272 + (8 * i16 + 4 * (g & 1)) * 2;
            PIN(clo); PIN(so); PIN(oo);
            __builtin_amdgcn_s_sleep(4);
            v4u fl2 = ld_c(ws, FL_OFF + lane * 16);
            BAR();
            wait_flags16(ws, FL_OFF + lane * 16, fl2, want);
            unsigned mu;
            for (unsigned s = 0; s < SPIN_CAP; ++s) {
                CBAR();
                mu = ld_c1(ws, SM_OFF + so);
                if (__all(mu != SENT)) break;
            }
            const float mv = asf(mu);
            const float M = max16(rowmax(mv));
            const float wf = __builtin_amdgcn_exp2f(mv - M);
            const unsigned wb = pk2(wf, wf);
            const unsigned keep = ((i16 >> 3) == (g >> 1)) ? hm : 0u;
            v4u af[4];
            {
                unsigned d0 = dppu<DPP_BC + 0>(wb) & keep, d1 = dppu<DPP_BC + 1>(wb) & keep, d2 = dppu<DPP_BC + 2>(wb) & keep, d3 = dppu<DPP_BC + 3>(wb) & keep;
#pragma unroll
                for (int q = 0; q < 4; ++q) {
                    af[0][q] = hq == q ? d0 : 0u; af[1][q] = hq == q ? d1 : 0u; af[2][q] = hq == q ? d2 : 0u; af[3][q] = hq == q ? d3 : 0u;
                }
                PIN(af[0]); PIN(af[1]); PIN(af[2]); PIN(af[3]);
            }
            BAR();
            STAMP(6);
            v4u o[4];
            unsigned lu;
            for (unsigned s = 0; s < SPIN_CAP; ++s) {
                CBAR();
#pragma unroll
                for (int j = 0; j < 4; ++j) o[j] = ld_c(ws, oo + j * 512);
                lu = ld_c1(ws, SL_OFF + so);
                if (__all(umax(umax(umax(mx4(o[0]), mx4(o[1])), umax(mx4(o[2]), mx4(o[3]))), lu) != SENT)) break;
            }
            STAMP(7);
            f32x4 d = Z4;
#pragma unroll
            for (int j = 0; j < 4; ++j) d = mfma32(af[j], o[j], d);
            const float Ls = add16(rowsum(bflo(wb) * asf(lu)));
            d *= __builtin_amdgcn_rcpf(Ls);
            const unsigned h0 = pk2(d[0], d[1]), h1 = pk2(d[2], d[3]);
            const unsigned l0 = pk2(d[0] - bflo(h0), d[1] - bfhi(h0)), l1 = pk2(d[2] - bflo(h1), d[3] - bfhi(h1));
            *(LAS v2u*)(L + clo) = v2u{h0, h1};
            *(LAS v2u*)(L + clo + 272) = v2u{l0, l1};
            BAR();
        } else {
            int pf[5];
#pragma unroll
            for (int i = 0; i < 4; ++i) {
                const int hh = 16 * hg + 4 * (w & 3) + i;
                pf[i] = OP_OFF + ((b * 32 + hh) * 4 + g) * 8192 + ((kb + 4 * hh + g) & 31) * 256 + i16 * 16;
                PIN(pf[i]);
            }
            {
                const int pc_ = CX_OFF + dh * 2048 + rg * 512 + dcq * 128 + c8 * 16;
                const int ps_ = SM_OFF + (lane >> 2) * 8192 + ((b * 32 + kb) * 32 + 16 * hg) * 4 + (lane & 3) * 16;
                pf[4] = lane < (w == 4 ? 32 : (w == 5 ? 8 : 0)) ? (w == 4 ? pc_ : ps_) : OOB;
                PIN(pf[4]);
            }
            SB();
            const void* wuvp = a.wuv;
            OPQ(wsp); OPQ(wuvp);
            const rsrc_t ws = mk(wsp, WS_BYTES);
#pragma unroll
            for (int i = 0; i < 5; ++i) st_c(ws, pf[i], SENT4);
            want = load_epoch(wsp);
            v4u fr[32];
            LAS unsigned char* tb = L + L_CK + (8 * g + tq) * 1024 + (w & 3) * 256 + (tp & 1) * 8;
            const int k0 = ((g & 1) << 3) | ((tq >> 1) << 2) | ((tq & 1) << 1), k1 = k0 ^ 9;
#define OFR(sg) _Pragma("unroll") for (int tau = 0; tau < 8; ++tau) \
                fr[tau * 4 + sg] = trd(tb + (((2 * tau + (tp >> 1)) ^ k0) * 16) + sg * 32768, tb + (((2 * tau + (tp >> 1)) ^ k1) * 16) + 4096 + sg * 32768);
            BAR(); OFR(0) BAR(); OFR(1) BAR(); OFR(2) fl = ld_c(ws, fo); BAR(); OFR(3)
            int ob[4];
#pragma unroll
            for (int rr = 0; rr < 4; ++rr) {
                const int hh = 16 * hg + 4 * g + rr;
                ob[rr] = OP_OFF + ((b * 32 + hh) * 4 + (w & 3)) * 8192 + ((kb + 4 * hh + (w & 3)) & 31) * 256 + i16 * 16;
                PIN(ob[rr]);
            }
            QGATHER();
            if (w == 7 && lane == 0) st_c2(ws, myfl, want);
            dma_loop(mks(wuvp, 512 * 4096 * 2), 8, rg * 8192 - (i16 & 8) * 16, i16, 0x66442200ull,
                     (128 * dcq + 64 * (w & 1)) * 8192 + dh * 256 + ((w >> 1) & 1) * 128, 8 * 8192, L_WV + (w & 3) * 8192);
            BAR();
            const f32x4 m4 = *(LAS f32x4*)(L + L_MX + i16 * 16);
            f32x4 o[8] = {Z4, Z4, Z4, Z4, Z4, Z4, Z4, Z4};
            f32x4 sv[8];
            v4u pp[4];
            LAS unsigned char* sp = L + L_S + i16 * SPITCH + g * 32;
#pragma unroll
            for (int i = 0; i < 8; ++i) sv[i] = *(LAS f32x4*)(sp + (i >> 1) * 128 + (i & 1) * 16);
            const float m = fmaxf(fmaxf(m4[0], m4[1]), fmaxf(m4[2], m4[3]));
#define PEXP(sg, q) pp[sg][q] = pk2(__builtin_amdgcn_exp2f(sv[2 * (sg) + ((q) >> 1)][2 * ((q) & 1)] - m), __builtin_amdgcn_exp2f(sv[2 * (sg) + ((q) >> 1)][2 * ((q) & 1) + 1] - m))
            PEXP(0, 0); PEXP(0, 1); PEXP(0, 2); PEXP(0, 3);
            SB();
#pragma unroll
            for (int sg = 0; sg < 4; ++sg) {
#pragma unroll
                for (int t2 = 0; t2 < 4; ++t2) {
                    o[2 * t2] = mfma32(pp[sg], fr[(2 * t2) * 4 + sg], o[2 * t2]);
                    o[2 * t2 + 1] = mfma32(pp[sg], fr[(2 * t2 + 1) * 4 + sg], o[2 * t2 + 1]);
                    if (sg < 3) PEXP(sg + 1, t2);
                    SB();
                }
            }
#pragma unroll
            for (int rr = 0; rr < 4; ++rr)
                st_c(ws, ob[rr], v4u{san(pk2(o[0][rr], o[1][rr])), san(pk2(o[2][rr], o[3][rr])), san(pk2(o[4][rr], o[5][rr])), san(pk2(o[6][rr], o[7][rr]))});
            if (w == 7) {
                f32x4 l = Z4;
#pragma unroll
                for (int sg = 0; sg < 4; ++sg) l = mfma32(ONES, pp[sg], l);
                st_c1(ws, lane < 16 ? SL_OFF + ((b * 32 + kb) * 32 + 16 * hg + i16) * 4 : OOB, asu(l[0]));
            }
            STAMP(5);
            BAR();
            VMW(4);
            BAR();
            v4u vf[16];
            {
                LAS unsigned char* vb = L + L_WV + (w & 1) * 16384 + (16 * tq + g) * 128 + tp * 8;
#pragma unroll
                for (int tau = 0; tau < 4; ++tau)
#pragma unroll
                    for (int s = 0; s < 4; ++s) {
                        LAS unsigned char* p = vb + ((tau ^ tq) * 32) + s * 512;
                        vf[tau * 4 + s] = trd(p, p + 8192);
                    }
            }
            int co = CX_OFF + dh * 2048 + (2 * (w & 1) + (g & 1)) * 512 + (2 * dcq + (g >> 1)) * 64 + i16 * 4;
            PIN(co);
            BAR();
            if (w < 6) {
                v4u af[4];
#pragma unroll
                for (int s = 0; s < 4; ++s) af[s] = *(LAS v4u*)(L + L_CL + i16 * 272 + (32 * s + 8 * g) * 2);
                f32x4 d[4] = {Z4, Z4, Z4, Z4};
#pragma unroll
                for (int s = 0; s < 4; ++s)
#pragma unroll
                    for (int tau = 0; tau < 4; ++tau) d[tau] = mfma32(af[s], vf[tau * 4 + s], d[tau]);
                const float e0 = rs16(sum4(d[0]), sum4(d[2])), e1 = rs16(sum4(d[1]), sum4(d[3]));
                st_c1(ws, co, san(pk2(e0, e1)));
            }
        }
        STAMP(8);
        DUMP();
        return;
    }

    const int ai = r - 16, h = dh, part = dcq;
    const bool isF = ai < 9;
    const int fu = 9 * xg + ai;
    const int e = ai - 9, kg = xg >> 1, et = (xg & 1) ? 5 + e : e;
    const bool isE = !isF && (e < ((xg & 1) ? 4 : 5));
    unsigned char* wsp = a.ws; const void* xp = a.x;
    if (w < 2) {
        __builtin_amdgcn_s_setprio(3);
        int xo = (lane >> 5) * 1152 + (lane & 31) * 36;
        const int ah = 4 * xg + ((ai + w) & 3), p8 = 2 * (ai >> 2) + w;
        int xe = ((i16 >> 3) * 576 + 72 * p8 + 18 * g) * 2;
        int qo = (72 * p8 + 18 * g) * 8192 + ah * 256 + i16 * 16;
        int qpo = QP_OFF + ah * 8192 + (i16 >> 2) * 2048 + (g & 1) * 1024 + ((g >> 1) * 8 + p8) * 64 + (i16 & 3) * 16;
        unsigned sl[4], sh[4];
        v4u ones;
#pragma unroll
        for (int q = 0; q < 4; ++q) {
            sl[q] = hq == q ? ((i16 & 1) ? 0x01000c0cu : 0x0c0c0100u) : 0x0c0c0c0cu;
            sh[q] = hq == q ? ((i16 & 1) ? 0x03020c0cu : 0x0c0c0302u) : 0x0c0c0c0cu;
            ones[q] = hq == q ? (hm & 0x3F803F80u) : 0u;
            PIN(sl[q]); PIN(sh[q]);
        }
        PIN(ones);
        PIN(xo); PIN(xe); PIN(qo); PIN(qpo);
        SB();
        const void* wqp = a.wq;
        OPQ(wsp); OPQ(xp); OPQ(wqp);
        const rsrc_t ws = mk(wsp, WS_BYTES);
        const rsrc_t rx = mk(xp, 2 * 576 * 2);
        const rsrc_t rq = mk(wqp, 576 * 4096 * 2);
        want = load_epoch(wsp);
        const v4u x0 = __builtin_amdgcn_raw_buffer_load_b128(rx, xo, 0, 0), x1 = __builtin_amdgcn_raw_buffer_load_b128(rx, xo + 16, 0, 0);
        const unsigned x2 = __builtin_amdgcn_raw_buffer_load_b32(rx, xo + 32, 0, 0);
        const v4u y0 = __builtin_amdgcn_raw_buffer_load_b128(rx, xe, 0, 0), y1 = __builtin_amdgcn_raw_buffer_load_b128(rx, xe + 16, 0, 0);
        const unsigned y2 = __builtin_amdgcn_raw_buffer_load_b32(rx, xe + 32, 0, 0);
        v4u wq[18];
#pragma unroll
        for (int j = 0; j < 18; ++j) wq[j] = __builtin_amdgcn_raw_buffer_load_b128(rq, qo, j * 8192, AUX_NT);
        SB();
        const unsigned xw[9] = {x0.x, x0.y, x0.z, x0.w, x1.x, x1.y, x1.z, x1.w, x2};
        const unsigned yw[9] = {y0.x, y0.y, y0.z, y0.w, y1.x, y1.y, y1.z, y1.w, y2};
        float s = 0.f, s2 = 0.f;
#pragma unroll
        for (int q = 0; q < 9; ++q) { s = dot2(xw[q], 0x3F803F80u, s); s2 = dot2(xw[q], xw[q], s2); }
        float mean = add16(rowsum(s)) * (1.0f / 576.0f);
        float rs = __builtin_amdgcn_rsqf(fmaxf(add16(rowsum(s2)) * (1.0f / 576.0f) - mean * mean, 0.f) + 1e-5f) * (0.08838834764831845f * 1.4426950408889634f);
        PIN(mean); PIN(rs);
        SB();
        STAMP(1);
        f32x4 acc = Z4, cs = Z4;
#pragma unroll
        for (int j = 0; j < 18; ++j) {
            v4u af;
#pragma unroll
            for (int q = 0; q < 4; ++q) af[q] = __builtin_amdgcn_perm(0u, yw[j >> 1], (j & 1) ? sh[q] : sl[q]);
            cs = mfma32(ones, wq[j], cs);
            acc = mfma32(af, wq[j], acc);
        }
        const f32x4 v = (acc - cs * mean) * rs;
        st_c(ws, qpo, v4u{pk2(v[0], v[1]), want.x, pk2(v[2], v[3]), want.x});
        __builtin_amdgcn_s_setprio(0);
        STAMP(2);
    } else {
        const bool slowF = isF && (fu & 7) == 3;
        int bx = xg, bai = ai, wb = w - 4;
        bool doB = (w == 4 || w == 5) && !slowF;
        if (w == 6 && !isF && !isE) {
            if (e >= 5) { bai = (3 - xg) & 7; wb = e - 5; doB = true; }
            else if (xg == 3 || xg == 1) { bx = 3; bai = 8; wb = xg >> 1; doB = true; }
        }
        if (doB) {
            const int h = 4 * bx + ((bai + 2 + wb) & 3), part = bai >> 2;
            int qa = QA_OFF + (((g >> 1) * 32 + h) * 512 + 128 * part + 64 * wb + 4 * i16 + 2 * (g & 1)) * 2;
            PIN(qa);
            SB();
            const void* wukp = a.wuk;
            OPQ(wsp); OPQ(wukp);
            const rsrc_t ws = mk(wsp, WS_BYTES);
            st_c1(ws, qa, SENT);
            want = load_epoch(wsp);
            __builtin_amdgcn_s_sleep(B_DELAY);
            dma_loop(mks(wukp, 512 * 4096 * 2), 16, g * 8192, i16, 0xFEDCBA9876543210ull, (128 * part + 64 * wb) * 8192 + h * 256, 4 * 8192,
                     L_UK + (w - 4) * 16384);
            VMW(16);
            if (lane == 0) st_c2(ws, FLQ_OFF + ((bx * 16 + bai) * 2 + wb) * 8, want);
            int po = QP_OFF + h * 8192 + i16 * 64 + g * 16;
            PIN(qa); PIN(po);
            VMW(1);
            STAMP(4);
            v4u qf[8];
#pragma unroll
            for (int j = 0; j < 8; ++j) qf[j] = ld_c(ws, po + j * 1024);
            v4u uk[16];
#pragma unroll
            for (int tau = 0; tau < 4; ++tau)
#pragma unroll
                for (int s = 0; s < 4; ++s) uk[tau * 4 + s] = *(LAS v4u*)(L + L_UK + (w - 4) * 16384 + i16 * 1024 + tau * 256 + (((4 * s + g) ^ i16) * 16));
            for (unsigned s = 0; s < SPIN_CAP; ++s) {
                unsigned dd = 0;
#pragma unroll
                for (int j = 0; j < 8; ++j) dd |= (qf[j].y ^ want.x) | (qf[j].w ^ want.x);
                if (__all(dd == 0u)) break;
                CBAR();
#pragma unroll
                for (int j = 0; j < 8; ++j) qf[j] = ld_c(ws, po + j * 1024);
            }
            STAMP(5);
            f32x4 d[4] = {Z4, Z4, Z4, Z4};
#pragma unroll
            for (int s = 0; s < 4; ++s) {
                const v4u q = {qf[2 * s].x, qf[2 * s].z, qf[2 * s + 1].x, qf[2 * s + 1].z};
#pragma unroll
                for (int tau = 0; tau < 4; ++tau) d[tau] = mfma32(q, uk[tau * 4 + s], d[tau]);
            }
            const float e0 = rs16(sum4(d[0]), sum4(d[2])), e1 = rs16(sum4(d[1]), sum4(d[3]));
            st_c1(ws, qa, san(pk2(e0, e1)));
            STAMP(6);
        } else {
            const int w3 = w < 4 ? w - 2 : (w < 6 ? 3 : w - 4);
            const int pf_ = FO_OFF + fu * 4608 + (w3 * 128 + lane) * 16;
            const int pe = AO_OFF + (2 * kg + rg) * 1152 + et * 128 + c8 * 16;
            const int lim = isF ? 288 - w3 * 128 : ((isE && w3 == 0) ? 16 : 0);
            int p0 = lane < lim ? (isF ? pf_ : pe) : OOB, p1 = lane + 64 < lim ? pf_ + 1024 : OOB;
            PIN(p0); PIN(p1);
            SB();
            OPQ(wsp);
            const rsrc_t ws = mk(wsp, WS_BYTES);
            st_c(ws, p0, SENT4);
            st_c(ws, p1, SENT4);
            want = load_epoch(wsp);
            VMW(0);
        }
    }
    OPQ(want);
    const rsrc_t ws = mk(wsp, WS_BYTES);
    const rsrc_t rx = mk(xp, 2 * 576 * 2);
    BAR();
    if (w == 7 && lane == 0) st_c2(ws, myfl, want);
    __builtin_amdgcn_s_sleep(1);

    if (isF) {
        if ((fu & 7) != 3) for (int z_ = 0; z_ < F_DELAY; ++z_) __builtin_amdgcn_s_sleep(8);
        {
            const int key = ((rg >> 1) & 1) | ((w & 1) << 1);
            dma_loop(mks(a.wd, 4608 * 576 * 2), 9, rg * 1152 - (i16 & 8) * 16, i16 ^ (key << 1), 0ull, (64 * fu + 8 * w) * 1152, 128, L_DN + w * 1024, 8192);
        }
        const int keyg = (((w & 3) >> 1) & 1) | (((rg >> 2) & 1) << 1);
        const v4u sgu = mks((w >> 2) ? a.wu : a.wg, 576 * 4608 * 2);
        const int sgo = 8 * (w & 3) * 9216 + fu * 128;
        dma_loop(sgu, 10, rg * 9216 - (i16 & 8) * 16, i16 ^ (keyg << 1), 0ull, sgo, 32 * 9216, L_GU1 + w * 1024, 8192);
        VMW(10);
        BAR();
        v4u df[10];
        {
            const int key = ((tq >> 1) & 1) | ((g & 1) << 1);
            LAS unsigned char* tb = L + L_DN + (w >> 2) * 8192 + (8 * g + tq) * 128 + tp * 8 + (((w & 3) ^ key) * 32);
#pragma unroll
            for (int i = 0; i < 5; ++i)
#pragma unroll
                for (int s = 0; s < 2; ++s) {
                    LAS unsigned char* p = tb + (i < 4 ? i : (w < 4 ? 4 : 0)) * 16384 + s * 4096;
                    df[i * 2 + s] = trd(p, p + 512);
                }
        }
        BAR();
        dma_loop(sgu, 8, rg * 9216 - (i16 & 8) * 16, i16 ^ (keyg << 1), 0ull, sgo + 10 * 32 * 9216, 32 * 9216, L_GU2 + w * 1024, 8192);
        VMW(8);
        BAR();
        v4u gf[18], uf[18];
        const int keyp = (tq & 1) | ((g & 1) << 1);
        const int tro = (16 * (tq & 1) + 4 * g + (tq >> 1)) * 128 + tp * 8 + (((w & 3) ^ keyp) * 32);
        if (w < 4) {
#pragma unroll
            for (int s = 0; s < 10; ++s) {
                LAS unsigned char* p = L + L_GU1 + s * 8192 + tro;
                gf[s] = trd(p, p + 256);
                uf[s] = trd(p + 4096, p + 4096 + 256);
            }
        }
        BAR();
        if (w >= 4) {
            const int t = tid - 256;
#pragma unroll
            for (int i = 0; i < 3; ++i) {
                const int k = t + 256 * i;
                if (k < 576) {
                    const int bb = k >= 288, kk = k - 288 * bb, m0 = bb * 576 + (kk >> 4) * 32 + (kk & 15);
                    const unsigned lo = __builtin_amdgcn_raw_buffer_load_b16(rx, m0 * 2, 0, 0), hi = __builtin_amdgcn_raw_buffer_load_b16(rx, (m0 + 16) * 2, 0, 0);
                    *(LAS unsigned*)(L + L_AO + (8 + bb) * AOP + kk * 4) = lo | (hi << 16);
                }
            }
#pragma unroll
            for (int i = 0; i < 2; ++i) {
                const int k = t + 256 * i;
                if (k < 6 * 73) *(LAS v4u*)(L + L_AO + 10 * AOP + k * 16) = v4u{0u, 0u, 0u, 0u};
            }
            if (w == 6) check_flags(ws, want, lane);
        }
        VMW(0);
        BAR();
        STAMP(7);
        float csg = 0.f, csu = 0.f;
        if (w < 4) {
#pragma unroll
            for (int s = 10; s < 18; ++s) {
                LAS unsigned char* p = L + L_GU2 + (s - 10) * 8192 + tro;
                gf[s] = trd(p, p + 256);
                uf[s] = trd(p + 4096, p + 4096 + 256);
            }
            f32x4 cg = Z4, cu = Z4;
#pragma unroll
            for (int s = 0; s < 18; ++s) { cg = mfma32(ONES, gf[s], cg); cu = mfma32(ONES, uf[s], cu); }
            csg = cg[0]; csu = cu[0];
            PIN(csg); PIN(csu);
        } else {
            const int wq_ = w - 4;
            int go[3], lo_[3];
#pragma unroll
            for (int i = 0; i < 3; ++i) {
                const int id = i * 64 + lane;
                go[i] = id < 144 ? AO_OFF + wq_ * 2304 + id * 16 : OOB;
                lo_[i] = id < 144 ? L_AO + wq_ * 2 * AOP + id * 16 + (id >= 72 ? 16 : 0) : L_JUNK + lane * 16;
                PIN(lo_[i]);
            }
            v4u av[3];
            for (unsigned s = 0; s < SPIN_CAP; ++s) {
                CBAR();
#pragma unroll
                for (int i = 0; i < 3; ++i) av[i] = ld_c(ws, go[i]);
                if (__all(umax(umax(mx4(av[0]), mx4(av[1])), mx4(av[2])) != SENT)) break;
            }
            STAMP(8);
#pragma unroll
            for (int i = 0; i < 3; ++i) *(LAS v4u*)(L + lo_[i]) = av[i];
        }
        int fo = i16 < 2 ? FO_OFF + fu * 4608 + w * 128 + i16 * 64 + g * 16 : OOB;
        PIN(fo);
        BAR();
        LAS unsigned char* ab = L + L_AO + i16 * AOP + g * 16;
        f32x4 dg = Z4, du = Z4;
        if (w < 4) {
            v4u af[6];
#pragma unroll
            for (int s = 0; s < 6; ++s) af[s] = *(LAS v4u*)(ab + s * 64);
            SB();
#pragma unroll
            for (int s = 0; s < 18; ++s) {
                const v4u cur = af[s % 6];
                dg = mfma32(cur, gf[s], dg);
                du = mfma32(cur, uf[s], du);
                if (s + 6 < 18) af[s % 6] = *(LAS v4u*)(ab + (s + 6) * 64);
                SB();
            }
        } else {
            const int wq_ = w - 4;
            v4u af[5];
#pragma unroll
            for (int i = 0; i < 5; ++i) af[i] = *(LAS v4u*)(ab + (i < 4 || wq_ < 2 ? wq_ + 4 * i : 0) * 64);
#pragma unroll
            for (int i = 0; i < 5; ++i) {
                if (i < 4 || wq_ < 2) {
                    dg = mfma32(af[i], af[i], dg);
                    du = mfma32(af[i], ONES, du);
                }
            }
            float s2 = (i16 & 1) ? dg[1] + dg[3] : dg[0] + dg[2];
            const float s1 = (i16 & 1) ? du[1] + du[3] : du[0] + du[2];
            s2 += dpp<DPP_ROR8>(s2); s2 += dpp<DPP_ROR4>(s2); s2 += dpp<DPP_ROR2>(s2);
            const float st = add32(rs16(s1, s2));
            if (lane < 32 && i16 < 2) *(LAS float*)(L + L_FS + ((i16 * 2 + g) * 4 + wq_) * 4) = st;
        }
        BAR();
        if (w < 4) {
            const f32x4 s1 = *(LAS f32x4*)(L + L_FS + (g >> 1) * 32), s2 = *(LAS f32x4*)(L + L_FS + (g >> 1) * 32 + 16);
            const float gs = add16(rs32(dg[0] + dg[2], dg[1] + dg[3])), us = add16(rs32(du[0] + du[2], du[1] + du[3]));
            const float mean = sum4(s1) * (1.0f / 576.0f);
            const float var = fmaxf(sum4(s2) * (1.0f / 576.0f) - mean * mean, 0.f);
            const float rstd = __builtin_amdgcn_rsqf(var + 1e-5f);
            const float gg = (gs - mean * csg) * rstd, uu = (us - mean * csu) * rstd;
            const float act = gg * uu * __builtin_amdgcn_rcpf(1.0f + __builtin_amdgcn_exp2f(gg * -1.4426950408889634f));
            if ((g & 1) == 0) *(LAS unsigned short*)(L + L_ACT + (g >> 1) * 128 + (16 * w + i16) * 2) = (unsigned short)pk2(act, act);
        }
        BAR();
        {
            const v4u b0 = *(LAS v4u*)(L + L_ACT + (i16 & 1) * 128 + g * 16), b1 = *(LAS v4u*)(L + L_ACT + (i16 & 1) * 128 + 64 + g * 16);
#pragma unroll
            for (int i = 0; i < 5; ++i) {
                if (i < 4 || w < 4) {
                    f32x4 d = mfma32(df[2 * i + 1], b1, mfma32(df[2 * i], b0, Z4));
                    st_c(ws, fo + i * 1024, __builtin_bit_cast(v4u, d));
                }
            }
        }
        STAMP(9);
        DUMP();
        return;
    }
    if (!isE) {
        if (blockIdx.x == 255 && w == 3) {
            check_flags(ws, want, lane);
            if (lane == 0) st_c2(ws, EPOCH_OFF, want);
        }
        DUMP();
        return;
    }
    {
        const int hh = 8 * kg + w;
        for (int z_ = 0; z_ < E_DELAY; ++z_) __builtin_amdgcn_s_sleep(8);
        dma_loop(mks(a.wo, 4096 * 576 * 2), 16, rg * 1152 - (i16 & 8) * 16, i16 ^ (((rg >> 2) & 1) << 2), 0x2200220022002200ull,
                 hh * 128 * 1152 + et * 128, 8 * 1152, L_WO + w * 16384);
        if (w == 6) check_flags(ws, want, lane);
        VMW(0);
        v4u wf[16];
        {
            const int keyp = (tq & 1) | ((g & 1) << 1);
            LAS unsigned char* tb = L + L_WO + w * 16384 + (16 * (tq & 1) + 4 * g + (tq >> 1)) * 128 + tp * 8;
#pragma unroll
            for (int tau = 0; tau < 4; ++tau)
#pragma unroll
                for (int s = 0; s < 4; ++s) {
                    LAS unsigned char* p = tb + ((tau ^ keyp) * 32) + s * 4096;
                    wf[tau * 4 + s] = trd(p, p + 256);
                }
        }
        int ao = AO_OFF + (2 * kg + (lane >> 5)) * 1152 + et * 128 + (lane & 31) * 4;
        PIN(ao);
        BAR();
        STAMP(7);
        v4u cf[4];
        const int co = i16 < 8 ? CX_OFF + hh * 2048 + i16 * 64 + g * 16 : OOB;
        for (unsigned s = 0; s < SPIN_CAP; ++s) {
            CBAR();
#pragma unroll
            for (int j = 0; j < 4; ++j) cf[j] = ld_c(ws, co + j * 512);
            if (__all(umax(umax(mx4(cf[0]), mx4(cf[1])), umax(mx4(cf[2]), mx4(cf[3]))) != SENT)) break;
        }
        STAMP(8);
        {
            f32x4 d[4] = {Z4, Z4, Z4, Z4};
#pragma unroll
            for (int s = 0; s < 4; ++s)
#pragma unroll
                for (int tau = 0; tau < 4; ++tau) d[tau] = mfma32(cf[s], wf[tau * 4 + s], d[tau]);
            const f32x4 ev = {rs16(d[0][0] + d[0][2], d[2][0] + d[2][2]), rs16(d[1][0] + d[1][2], d[3][0] + d[3][2]),
                              rs16(d[0][1] + d[0][3], d[2][1] + d[2][3]), rs16(d[1][1] + d[1][3], d[3][1] + d[3][3])};
            if (lane < 32) *(LAS f32x4*)(L + L_RE + w * 512 + lane * 16) = ev;
        }
        BAR();
        const int ru = 9 * kg + et;
        if (w == 0) {
            LAS unsigned char* p = L + L_RE + (lane >> 5) * 2048 + (lane & 31) * 16;
            const f32x4 v = (*(LAS f32x4*)p + *(LAS f32x4*)(p + 512)) + (*(LAS f32x4*)(p + 1024) + *(LAS f32x4*)(p + 1536));
            st_c1(ws, ao, san(pk2(rs32(v[0], v[2]), rs32(v[1], v[3]))));
            STAMP(9);
        } else if (w == 1) {
            const v2u xr = __builtin_amdgcn_raw_buffer_load_b64(rx, rg == 4 ? ((c8 >> 2) * 576 + 16 * ru + 4 * (c8 & 3)) * 2 : OOB, 0, 0);
            __builtin_amdgcn_s_sleep(R_DELAY);
            v4u hv;
            const int ho = rg < 4 ? AO_OFF + (2 * rg + (c8 >> 2)) * 1152 + (ru >> 1) * 64 + (c8 & 3) * 16 : OOB;
            for (unsigned s = 0; s < SPIN_CAP; ++s) {
                CBAR();
                hv = ld_c(ws, ho);
                if (__all(mx4(hv) != SENT)) break;
            }
            f32x4 acc = {bflo(xr.x), bfhi(xr.x), bflo(xr.y), bfhi(xr.y)};
#pragma unroll
            for (int j = 0; j < 4; ++j) acc[j] += (ru & 1) ? bfhi(hv[j]) : bflo(hv[j]);
            PIN(acc);
            v4u fv[9];
            const int fo = FO_OFF + rg * 4608 + ru * 128 + c8 * 16;
            for (unsigned s = 0; s < SPIN_CAP; ++s) {
                CBAR();
#pragma unroll
                for (int j = 0; j < 9; ++j) fv[j] = ld_c(ws, fo + j * 8 * 4608);
                unsigned m = mx4(fv[0]);
#pragma unroll
                for (int j = 1; j < 9; ++j) m = umax(m, mx4(fv[j]));
                if (__all(m != SENT)) break;
            }
            STAMP(10);
#pragma unroll
            for (int j = 0; j < 9; ++j) acc += __builtin_bit_cast(f32x4, fv[j]);
            float u0 = rs32(acc[0], acc[2]), u1 = rs32(acc[1], acc[3]);
            u0 = add16(u0); u1 = add16(u1);
            u0 += dpp<DPP_ROR8>(u0); u1 += dpp<DPP_ROR8>(u1);
            __builtin_amdgcn_raw_buffer_store_b32(pk2(u0, u1), mk(a.out, 2304), (lane & 31) < 8 ? ((c8 >> 2) * 576 + 16 * ru + 4 * (c8 & 3) + 2 * (lane >> 5)) * 2 : OOB, 0, 0);
            STAMP(11);
        }
        DUMP();
    }
}

}

extern "C" void kernel_launch(void* const* d_in, const int* in_sizes, int n_in, void* d_out, int out_size, void* d_ws,
                              size_t ws_size, hipStream_t stream) {
    Args a;
    a.x = d_in[0]; a.ckv = d_in[1]; a.wq = d_in[2]; a.wuk = d_in[3]; a.wuv = d_in[4]; a.wo = d_in[5];
    a.wg = d_in[7]; a.wu = d_in[8]; a.wd = d_in[9];
    a.out = d_out; a.ws = (unsigned char*)d_ws;
    (void)hipFuncSetAttribute((const void*)fused, hipFuncAttributeMaxDynamicSharedMemorySize, LDS_BYTES);
    hipLaunchKernelGGL(fused, dim3(256), dim3(NT), LDS_BYTES, stream, a);
}
```

```cpp
#include <hip/hip_runtime.h>
#include <hip/hip_bf16.h>

#define LAS __attribute__((address_space(3)))
typedef unsigned v4u __attribute__((ext_vector_type(4)));
typedef unsigned v2u __attribute__((ext_vector_type(2)));
typedef float f32x4 __attribute__((ext_vector_type(4)));
typedef float f32x2 __attribute__((ext_vector_type(2)));
typedef short s16x4 __attribute__((ext_vector_type(4)));
typedef __bf16 bf16x8 __attribute__((ext_vector_type(8)));
typedef __bf16 bf16x2 __attribute__((ext_vector_type(2)));
typedef __amdgpu_buffer_rsrc_t rsrc_t;
#define DI __device__ __forceinline__

#pragma clang diagnostic ignored "-Winline-asm"
namespace {

constexpr int NT = 512;
constexpr unsigned SENT = 0xFFFFFFFFu;
constexpr int AUX_NT = 2, AUX_SC1 = 16;
constexpr unsigned SPIN_CAP = 1u << 18;
constexpr int OOB = 0x7ffff000;

constexpr int EPOCH_OFF = 0;
constexpr int FL_OFF = 1024;
constexpr int FLQ_OFF = 4096;
constexpr int QP_OFF = 8192;
constexpr int QA_OFF = QP_OFF + 262144;
constexpr int SM_OFF = QA_OFF + 65536;
constexpr int SL_OFF = SM_OFF + 8192;
constexpr int CX_OFF = SL_OFF + 8192;
constexpr int AO_OFF = CX_OFF + 65536;
constexpr int FO_OFF = AO_OFF + 9216;
constexpr int OP_OFF = 1 << 20;
constexpr int WS_BYTES = OP_OFF + (2 << 20);

constexpr int L_CK = 0;
constexpr int L_Q = 131072;
constexpr int SPITCH = 528;
constexpr int L_S = L_Q + 16384;
constexpr int L_MX = L_S + 16 * SPITCH;
constexpr int L_WV = 0;
constexpr int L_CL = L_Q;
constexpr int L_UK = 0;
constexpr int L_DN = 0;
constexpr int L_GU1 = 73728;
constexpr int L_GU2 = 0;
constexpr int L_AO = 73728;
constexpr int AOP = 1168;
constexpr int L_FS = 155648;
constexpr int L_ACT = L_FS + 64;
constexpr int L_WO = 0;
constexpr int L_RE = 131072;
constexpr int L_JUNK = 156672;
constexpr int LDS_BYTES = L_JUNK + 1024;
static_assert(L_MX + 256 <= L_JUNK && L_ACT + 256 <= L_JUNK, "lds map");
__host__ __device__ constexpr int ckey(int row) { return ((((row >> 3) ^ (row >> 2)) & 1) << 3) | (((row >> 1) & 1) << 2) | ((row & 1) << 1) | ((row >> 2) & 1); }
constexpr unsigned long long ckey_tab() { unsigned long long t = 0; for (int i = 0; i < 16; ++i) t |= (unsigned long long)ckey(i) << (4 * i); return t; }

DI v4u ld_c(rsrc_t r, int off) { return __builtin_amdgcn_raw_buffer_load_b128(r, off, 0, AUX_SC1); }
DI unsigned ld_c1(rsrc_t r, int off) { return __builtin_amdgcn_raw_buffer_load_b32(r, off, 0, AUX_SC1); }
DI void st_c(rsrc_t r, int off, v4u v) { __builtin_amdgcn_raw_buffer_store_b128(v, r, off, 0, AUX_SC1); }
DI void st_c2(rsrc_t r, int off, v2u v) { __builtin_amdgcn_raw_buffer_store_b64(v, r, off, 0, AUX_SC1); }
DI void st_c1(rsrc_t r, int off, unsigned v) { __builtin_amdgcn_raw_buffer_store_b32(v, r, off, 0, AUX_SC1); }
DI unsigned san(unsigned x) { return x < 0xFFFFFFFEu ? x : 0xFFFFFFFEu; }
DI rsrc_t mk(const void* p, unsigned bytes) { return __builtin_amdgcn_make_buffer_rsrc((void*)p, 0, bytes, 0x00020000); }
DI v4u mks(const void* p, unsigned bytes) {
    const unsigned long long a = (unsigned long long)p;
    return v4u{(unsigned)a, (unsigned)(a >> 32) & 0xFFFFu, bytes, 0x00020000u};
}
DI void dma(v4u srd, int voff, int soff, int lds) {
    asm volatile("s_mov_b32 m0, %0\n\ts_nop 0\n\tbuffer_load_dwordx4 %1, %2, %3 offen nt lds" ::"s"(lds), "v"(voff), "s"(srd), "s"(soff) : "m0", "memory");
}
DI void dmap(v4u srd, int voff, int soff, int lds) {
    asm volatile("s_mov_b32 m0, %0\n\ts_nop 0\n\tbuffer_load_dwordx4 %1, %2, %3 offen lds" ::"s"(lds), "v"(voff), "s"(srd), "s"(soff) : "m0", "memory");
}
DI float asf(unsigned u) { return __builtin_bit_cast(float, u); }
DI unsigned asu(float f) { return __builtin_bit_cast(unsigned, f); }
DI float bflo(unsigned u) { return asf(u << 16); }
DI float bfhi(unsigned u) { return asf(u & 0xFFFF0000u); }
DI unsigned pk2(float a, float b) { bf16x2 t = {(__bf16)a, (__bf16)b}; return __builtin_bit_cast(unsigned, t); }
DI float dot2(unsigned a, unsigned b, float c) { return __builtin_amdgcn_fdot2_f32_bf16(__builtin_bit_cast(bf16x2, a), __builtin_bit_cast(bf16x2, b), c, false); }
DI f32x4 mfma32(v4u a, v4u b, f32x4 c) {
    return __builtin_amdgcn_mfma_f32_16x16x32_bf16(__builtin_bit_cast(bf16x8, a), __builtin_bit_cast(bf16x8, b), c, 0, 0, 0);
}
template <int CTRL> DI float dpp(float v) { return asf((unsigned)__builtin_amdgcn_update_dpp(0, (int)asu(v), CTRL, 0xf, 0xf, true)); }
template <int CTRL> DI unsigned dppu(unsigned v) { return (unsigned)__builtin_amdgcn_update_dpp(0, (int)v, CTRL, 0xf, 0xf, true); }
constexpr int DPP_ROR8 = 0x128, DPP_ROR4 = 0x124, DPP_ROR2 = 0x122, DPP_ROR1 = 0x121;
constexpr int DPP_BC = 0x150;
DI float rs16(float a, float b) { auto r = __builtin_amdgcn_permlane16_swap(asu(a), asu(b), false, false); unsigned x = r[0], y = r[1]; return asf(x) + asf(y); }
DI float rs32(float a, float b) { auto r = __builtin_amdgcn_permlane32_swap(asu(a), asu(b), false, false); unsigned x = r[0], y = r[1]; return asf(x) + asf(y); }
DI float add16(float v) { return rs16(v, v); }
DI float add32(float v) { return rs32(v, v); }
DI float max16(float v) { auto r = __builtin_amdgcn_permlane16_swap(asu(v), asu(v), false, false); unsigned a = r[0], b = r[1]; return fmaxf(asf(a), asf(b)); }
DI float max32(float v) { auto r = __builtin_amdgcn_permlane32_swap(asu(v), asu(v), false, false); unsigned a = r[0], b = r[1]; return fmaxf(asf(a), asf(b)); }
DI float rowsum(float v) { v += dpp<DPP_ROR8>(v); v += dpp<DPP_ROR4>(v); v += dpp<DPP_ROR2>(v); v += dpp<DPP_ROR1>(v); return v; }
DI float rowmax(float v) { v = fmaxf(v, dpp<DPP_ROR8>(v)); v = fmaxf(v, dpp<DPP_ROR4>(v)); v = fmaxf(v, dpp<DPP_ROR2>(v)); v = fmaxf(v, dpp<DPP_ROR1>(v)); return v; }
DI float sum4(f32x4 d) { return (d[0] + d[2]) + (d[1] + d[3]); }
DI unsigned umax(unsigned a, unsigned b) { return a > b ? a : b; }
DI unsigned mx4(v4u v) { return umax(umax(v.x, v.y), umax(v.z, v.w)); }
DI v4u trd(LAS unsigned char* p0, LAS unsigned char* p1) {
    v2u a = __builtin_bit_cast(v2u, __builtin_amdgcn_ds_read_tr16_b64_v4i16((LAS s16x4*)p0));
    v2u b = __builtin_bit_cast(v2u, __builtin_amdgcn_ds_read_tr16_b64_v4i16((LAS s16x4*)p1));
    return v4u{a.x, a.y, b.x, b.y};
}
#define ALL(c) (__builtin_amdgcn_ballot_w64(!(c)) == 0ull)
#define CBAR() asm volatile("" ::: "memory")
#define VMW(n) asm volatile("s_waitcnt vmcnt(" #n ")" ::: "memory")
#define BAR() asm volatile("s_waitcnt lgkmcnt(0)\n\ts_barrier" ::: "memory")
#define PIN(v) asm volatile("" : "+v"(v))
#define SB() __builtin_amdgcn_sched_barrier(0)
#define OPQ(p) asm volatile("" : "+s"(p))
constexpr f32x4 Z4 = {0.f, 0.f, 0.f, 0.f};

DI int st_off(int b, int h, int kb) { return ((b * 8 + (h >> 2)) * 4 + (kb >> 3)) * 128 + (kb & 7) * 16 + (h & 3) * 4; }
DI void check_flags(rsrc_t ws, v2u want, int lane) {
    for (unsigned s = 0; s < SPIN_CAP; ++s) {
        CBAR();
        v4u a = ld_c(ws, FL_OFF + lane * 16), b = ld_c(ws, FL_OFF + 1024 + lane * 16);
        unsigned d = (a.x ^ want.x) | (a.y ^ want.y) | (a.z ^ want.x) | (a.w ^ want.y) | (b.x ^ want.x) | (b.y ^ want.y) | (b.z ^ want.x) | (b.w ^ want.y);
        if (ALL(d == 0u)) break;
    }
    CBAR();
}
DI v2u load_epoch(const unsigned char* ws) {
    const unsigned long long e = *(const __attribute__((address_space(4))) unsigned long long*)(ws + EPOCH_OFF) + 1ull;
    return v2u{(unsigned)e, (unsigned)(e >> 32)};
}

struct Args {
    const void *x, *ckv, *wq, *wuk, *wuv, *wo, *wg, *wu, *wd;
    void* out;
    unsigned char* ws;
};
constexpr int B_DELAY = 10;
constexpr int E_DELAY = 5;
constexpr int F_DELAY = 2;
constexpr int H_S0 = 6;
constexpr int FBAR = 12;
constexpr int R_DELAY = 20;
constexpr int CODE_KB = 21;

DI void dma_loop(v4u srd, int n, int vhi, int i16, unsigned long long tab, int soff, int sstep, int lds, int lstep = 1024) {
    soff = __builtin_amdgcn_readfirstlane(soff); lds = __builtin_amdgcn_readfirstlane(lds);
    asm volatile("s_nop 4" : "+s"(soff), "+s"(lds));
#pragma unroll 1
    for (int i = 0; i < n; ++i) {
        dma(srd, vhi + ((i16 ^ (int)((unsigned)tab & 15u)) << 4), soff, lds);
        tab >>= 4; soff += sstep; lds += lstep;
    }
}
DI void wait_flags16(rsrc_t ws, int off, v4u fl, v2u want) {
    for (unsigned s = 0; s < SPIN_CAP; ++s) {
        if (ALL(((fl.x ^ want.x) | (fl.y ^ want.y) | (fl.z ^ want.x) | (fl.w ^ want.y)) == 0u)) break;
        CBAR();
        fl = ld_c(ws, off);
    }
    CBAR();
}

__global__ void __launch_bounds__(NT, 2) fused(Args a) {
    extern __shared__ __attribute__((aligned(16))) unsigned char smem[];
    LAS unsigned char* L = (LAS unsigned char*)smem;
    const int tid = threadIdx.x, lane = tid & 63, w = __builtin_amdgcn_readfirstlane(tid >> 6);
    const int xg = blockIdx.x & 7, r = blockIdx.x >> 3;
    const int c8 = lane & 7, rg = lane >> 3, i16 = lane & 15, g = lane >> 4;
    const int tq = i16 >> 2, tp = i16 & 3;
    const v4u SENT4 = {SENT, SENT, SENT, SENT};
    const int dh = 8 * (xg >> 1) + (r & 7), dcq = 2 * (xg & 1) + ((r >> 3) & 1);
    const unsigned hm = (i16 & 1) ? 0xFFFF0000u : 0x0000FFFFu;
    const int hq = (i16 >> 1) & 3;
    const v4u ONES = {0x3F803F80u, 0x3F803F80u, 0x3F803F80u, 0x3F803F80u};
    if (w == 7 && r < CODE_KB) {
        unsigned long long pc;
        asm volatile("s_getpc_b64 %0" : "=s"(pc));
        pc &= ~0xFFull;
        const v4u sc = v4u{(unsigned)pc, (unsigned)(pc >> 32) & 0xFFFFu, (unsigned)(CODE_KB * 1024), 0x00020000u};
        dmap(sc, lane * 16, r * 1024, L_JUNK);
    }
    v2u want;

    const int myfl = FL_OFF + ((r >> 4) * 128 + xg * 16 + (r & 15)) * 8;
    if (r < 16) {
        const int b = xg >> 2, kb = 8 * (xg & 3) + (r >> 1), hg = r & 1;
        unsigned char* wsp = a.ws;
        const int fo = FLQ_OFF + (w * 16 + i16) * 16;
        v4u fl;
        int ql0 = L_Q + w * 1024 + ((lane ^ ckey(w)) * 16), ql1 = L_Q + (8 + w) * 1024 + ((lane ^ ckey(8 + w)) * 16);
        int qoff = QA_OFF + (b * 32 + 16 * hg) * 1024 + tid * 16;
        v4u q0, q1;
#define LAST() do { VMW(0); OPQ(want); wait_flags16(ws, fo, fl, want); BAR(); \
        q0 = ld_c(ws, qoff); q1 = ld_c(ws, qoff + 8192); SB(); } while (0)
#define QGATHER() \
        PIN(ql0); PIN(ql1); \
        for (unsigned s = 0; s < SPIN_CAP; ++s) { \
            if (ALL(umax(mx4(q0), mx4(q1)) != SENT)) break; \
            CBAR(); \
            q0 = ld_c(ws, qoff); q1 = ld_c(ws, qoff + 8192); \
        } \
        *(LAS v4u*)(L + ql0) = q0; \
        *(LAS v4u*)(L + ql1) = q1; \
        BAR();
        if (w < 4) {
            const int key = ckey(i16);
            v4u fr[32];
            const unsigned long long tab = (w & 1) ? (ckey_tab() >> 32) : ckey_tab();
            const int cso = (b * 4096 + 128 * kb + 8 * w) * 1024;
            int vhi = g * 256;
            PIN(vhi);
            SB();
            const void* ckvp = a.ckv;
            OPQ(wsp); OPQ(ckvp);
            const rsrc_t ws = mk(wsp, WS_BYTES);
            const v4u sck = mks(ckvp, 2 * 4096 * 512 * 2);
#define CKV(sg) dma_loop(sck, 8, vhi, i16, tab, cso + sg * 32768, 1024, L_CK + sg * 32768 + w * 8192)
#define SFR() do { LAS unsigned char* sb = L + L_CK + (32 * w + i16) * 1024; \
                _Pragma("unroll") for (int s = 0; s < 16; ++s) { \
                    LAS unsigned char* p = sb + ((((4 * s + g) & 15) ^ key) * 16) + (s >> 2) * 256; \
                    fr[s] = *(LAS v4u*)p; fr[16 + s] = *(LAS v4u*)(p + 16384); } } while (0)
#define LSTEP0() do { VMW(16); BAR(); CKV(3); } while (0)
#define LSTEP1() do { VMW(16); BAR(); } while (0)
#define LSTEP2() do { fl = ld_c(ws, fo); VMW(9); BAR(); } while (0)
#define LSTEP3() LAST()
            want = load_epoch(wsp);
            CKV(0); CKV(1); CKV(2);
            OPQ(want);
            LSTEP0();
            if (w > 0) { LSTEP1(); if (w > 1) { LSTEP2(); if (w > 2) LSTEP3(); } }
            SFR();
            if (w < 1) LSTEP1();
            if (w < 2) LSTEP2();
            if (w < 3) LSTEP3();
            QGATHER();
            const v4u fl2 = ld_c(ws, FL_OFF + lane * 16);
            f32x4 s0 = Z4, s1 = Z4;
            LAS unsigned char* qb = L + L_Q + i16 * 1024;
            v4u qf[16];
#pragma unroll
            for (int s = 0; s < 16; ++s) qf[s] = *(LAS v4u*)(qb + ((((4 * s + g) & 15) ^ key) * 16) + (s >> 2) * 256);
            SB();
#pragma unroll
            for (int s = 0; s < 16; ++s) {
                s0 = mfma32(fr[s], qf[s], s0);
                s1 = mfma32(fr[16 + s], qf[s], s1);
            }
            LAS unsigned char* sp = L + L_S + i16 * SPITCH + (32 * w + 4 * g) * 4;
            *(LAS f32x4*)sp = s0;
            *(LAS f32x4*)(sp + 64) = s1;
            float mx = fmaxf(fmaxf(fmaxf(s0[0], s0[1]), fmaxf(s0[2], s0[3])), fmaxf(fmaxf(s1[0], s1[1]), fmaxf(s1[2], s1[3])));
            mx = max32(max16(mx));
            if (lane < 16) *(LAS float*)(L + L_MX + (i16 * 4 + w) * 4) = mx;
            BAR();
            if (w == 0) {
                const f32x4 m4 = *(LAS f32x4*)(L + L_MX + i16 * 16);
                const float m = fmaxf(fmaxf(m4[0], m4[1]), fmaxf(m4[2], m4[3]));
                st_c1(ws, lane < 16 ? SM_OFF + st_off(b, 16 * hg + i16, kb) : OOB, asu(m));
            }
            const int kbl = (2 * ((i16 + 4 * w) & 15) + (g & 1) - 4 * dh - dcq) & 31;
            int so = st_off(g >> 1, dh, kbl);
            int oo = OP_OFF + (((g >> 1) * 32 + dh) * 4 + dcq) * 8192 + (8 * w + (g & 1)) * 256 + i16 * 16;
            int clo = L_CL + ((g >> 1) * 8 + 2 * w) * 272 + (8 * i16 + 4 * (g & 1)) * 2;
            PIN(clo); PIN(so); PIN(oo);
            wait_flags16(ws, FL_OFF + lane * 16, fl2, want);
            BAR();
            unsigned mu;
            for (unsigned s = 0; s < SPIN_CAP; ++s) {
                CBAR();
                mu = ld_c1(ws, SM_OFF + so);
                if (ALL(mu != SENT)) break;
            }
            const float mv = asf(mu);
            const float M = max16(rowmax(mv));
            const float wf = __builtin_amdgcn_exp2f(mv - M);
            const unsigned wb = pk2(wf, wf);
            const unsigned keep = ((i16 >> 3) == (g >> 1)) ? hm : 0u;
            v4u af[4];
            {
                unsigned d0 = dppu<DPP_BC + 0>(wb) & keep, d1 = dppu<DPP_BC + 1>(wb) & keep, d2 = dppu<DPP_BC + 2>(wb) & keep, d3 = dppu<DPP_BC + 3>(wb) & keep;
#pragma unroll
                for (int q = 0; q < 4; ++q) {
                    af[0][q] = hq == q ? d0 : 0u; af[1][q] = hq == q ? d1 : 0u; af[2][q] = hq == q ? d2 : 0u; af[3][q] = hq == q ? d3 : 0u;
                }
                PIN(af[0]); PIN(af[1]); PIN(af[2]); PIN(af[3]);
            }
            BAR();
            v4u o[4];
            unsigned lu;
            for (unsigned s = 0; s < SPIN_CAP; ++s) {
                CBAR();
#pragma unroll
                for (int j = 0; j < 4; ++j) o[j] = ld_c(ws, oo + j * 512);
                lu = ld_c1(ws, SL_OFF + so);
                if (ALL(umax(umax(umax(mx4(o[0]), mx4(o[1])), umax(mx4(o[2]), mx4(o[3]))), lu) != SENT)) break;
            }
            f32x4 d = Z4;
#pragma unroll
            for (int j = 0; j < 4; ++j) d = mfma32(af[j], o[j], d);
            const float Ls = add16(rowsum(bflo(wb) * asf(lu)));
            d *= __builtin_amdgcn_rcpf(Ls);
            const unsigned h0 = pk2(d[0], d[1]), h1 = pk2(d[2], d[3]);
            const unsigned l0 = pk2(d[0] - bflo(h0), d[1] - bfhi(h0)), l1 = pk2(d[2] - bflo(h1), d[3] - bfhi(h1));
            *(LAS v2u*)(L + clo) = v2u{h0, h1};
            *(LAS v2u*)(L + clo + 272) = v2u{l0, l1};
            BAR();
        } else {
            int pf[5];
#pragma unroll
            for (int i = 0; i < 4; ++i) {
                const int hh = 16 * hg + 4 * (w & 3) + i;
                pf[i] = OP_OFF + ((b * 32 + hh) * 4 + g) * 8192 + ((kb + 4 * hh + g) & 31) * 256 + i16 * 16;
                PIN(pf[i]);
            }
            {
                const int pc_ = CX_OFF + dh * 2048 + rg * 512 + dcq * 128 + c8 * 16;
                const int ps_ = SM_OFF + (lane >> 2) * 8192 + st_off(b, 16 * hg + 4 * (lane & 3), kb);
                pf[4] = lane < (w == 4 ? 32 : (w == 5 ? 8 : 0)) ? (w == 4 ? pc_ : ps_) : OOB;
                PIN(pf[4]);
            }
            SB();
            const void* wuvp = a.wuv;
            OPQ(wsp); OPQ(wuvp);
            const rsrc_t ws = mk(wsp, WS_BYTES);
#define PFILL() _Pragma("unroll") for (int i = 0; i < 5; ++i) st_c(ws, pf[i], SENT4);
            PFILL()
            want = load_epoch(wsp);
            v4u fr[32];
            LAS unsigned char* tb = L + L_CK + (8 * g + tq) * 1024 + (w & 3) * 256 + (tp & 1) * 8;
            const int k0 = ((g & 1) << 3) | ((tq >> 1) << 2) | ((tq & 1) << 1), k1 = k0 ^ 9;
#define OFR(sg) _Pragma("unroll") for (int tau = 0; tau < 8; ++tau) \
                fr[tau * 4 + sg] = trd(tb + (((2 * tau + (tp >> 1)) ^ k0) * 16) + sg * 32768, tb + (((2 * tau + (tp >> 1)) ^ k1) * 16) + 4096 + sg * 32768);
            OPQ(want);
            VMW(0);
            BAR();
            if (w == 7 && lane == 0) st_c2(ws, myfl, want);
            OFR(0) BAR(); OFR(1) fl = ld_c(ws, fo); BAR(); OFR(2) LAST(); OFR(3)
            int ob[4];
#pragma unroll
            for (int rr = 0; rr < 4; ++rr) {
                const int hh = 16 * hg + 4 * g + rr;
                ob[rr] = OP_OFF + ((b * 32 + hh) * 4 + (w & 3)) * 8192 + ((kb + 4 * hh + (w & 3)) & 31) * 256 + i16 * 16;
                PIN(ob[rr]);
            }
            QGATHER();
            dma_loop(mks(wuvp, 512 * 4096 * 2), 8, rg * 8192 - (i16 & 8) * 16, i16, 0x66442200ull,
                     (128 * dcq + 64 * (w & 1)) * 8192 + dh * 256 + ((w >> 1) & 1) * 128, 8 * 8192, L_WV + (w & 3) * 8192);
            BAR();
            const f32x4 m4 = *(LAS f32x4*)(L + L_MX + i16 * 16);
            f32x4 o[8] = {Z4, Z4, Z4, Z4, Z4, Z4, Z4, Z4};
            f32x4 sv[8];
            v4u pp[4];
            LAS unsigned char* sp = L + L_S + i16 * SPITCH + g * 32;
#pragma unroll
            for (int i = 0; i < 8; ++i) sv[i] = *(LAS f32x4*)(sp + (i >> 1) * 128 + (i & 1) * 16);
            const float m = fmaxf(fmaxf(m4[0], m4[1]), fmaxf(m4[2], m4[3]));
#define PEXP(sg, q) pp[sg][q] = pk2(__builtin_amdgcn_exp2f(sv[2 * (sg) + ((q) >> 1)][2 * ((q) & 1)] - m), __builtin_amdgcn_exp2f(sv[2 * (sg) + ((q) >> 1)][2 * ((q) & 1) + 1] - m))
            PEXP(0, 0); PEXP(0, 1); PEXP(0, 2); PEXP(0, 3);
            SB();
#pragma unroll
            for (int sg = 0; sg < 4; ++sg) {
#pragma unroll
                for (int t2 = 0; t2 < 4; ++t2) {
                    o[2 * t2] = mfma32(pp[sg], fr[(2 * t2) * 4 + sg], o[2 * t2]);
                    o[2 * t2 + 1] = mfma32(pp[sg], fr[(2 * t2 + 1) * 4 + sg], o[2 * t2 + 1]);
                    if (sg < 3) PEXP(sg + 1, t2);
                    SB();
                }
            }
#pragma unroll
            for (int rr = 0; rr < 4; ++rr)
                st_c(ws, ob[rr], v4u{san(pk2(o[0][rr], o[1][rr])), san(pk2(o[2][rr], o[3][rr])), san(pk2(o[4][rr], o[5][rr])), san(pk2(o[6][rr], o[7][rr]))});
            if (w == 7) {
                f32x4 l = Z4;
#pragma unroll
                for (int sg = 0; sg < 4; ++sg) l = mfma32(ONES, pp[sg], l);
                st_c1(ws, lane < 16 ? SL_OFF + st_off(b, 16 * hg + i16, kb) : OOB, asu(l[0]));
            }
            BAR();
            VMW(4);
            BAR();
            v4u vf[16];
            {
                LAS unsigned char* vb = L + L_WV + (w & 1) * 16384 + (16 * tq + g) * 128 + tp * 8;
#pragma unroll
                for (int tau = 0; tau < 4; ++tau)
#pragma unroll
                    for (int s = 0; s < 4; ++s) {
                        LAS unsigned char* p = vb + ((tau ^ tq) * 32) + s * 512;
                        vf[tau * 4 + s] = trd(p, p + 8192);
                    }
            }
            int co = CX_OFF + dh * 2048 + (2 * (w & 1) + (g & 1)) * 512 + (2 * dcq + (g >> 1)) * 64 + i16 * 4;
            PIN(co);
            BAR();
            if (w < 6) {
                v4u af[4];
#pragma unroll
                for (int s = 0; s < 4; ++s) af[s] = *(LAS v4u*)(L + L_CL + i16 * 272 + (32 * s + 8 * g) * 2);
                f32x4 d[4] = {Z4, Z4, Z4, Z4};
#pragma unroll
                for (int s = 0; s < 4; ++s)
#pragma unroll
                    for (int tau = 0; tau < 4; ++tau) d[tau] = mfma32(af[s], vf[tau * 4 + s], d[tau]);
                const float e0 = rs16(sum4(d[0]), sum4(d[2])), e1 = rs16(sum4(d[1]), sum4(d[3]));
                st_c1(ws, co, san(pk2(e0, e1)));
            }
        }
        return;
    }

    const int ai = r - 16;
    const bool isF = ai < 9;
    const int fu = 9 * xg + ai;
    const int e = ai - 9, kg = xg >> 1, et = (xg & 1) ? 5 + e : e;
    const bool isE = !isF && (e < ((xg & 1) ? 4 : 5));
    unsigned char* wsp = a.ws; const void* xp = a.x;
    if (w < 2) {
        __builtin_amdgcn_s_setprio(3);
        int xo = (lane >> 5) * 1152 + (lane & 31) * 36;
        const int ah = 8 * (xg >> 1) + ((2 * ai + w) & 7), p8 = 4 * (xg & 1) + (ai >> 2);
        int xe = ((i16 >> 3) * 576 + 72 * p8 + 18 * g) * 2;
        int qo = (72 * p8 + 18 * g) * 8192 + ah * 256 + i16 * 16;
        int qpo = QP_OFF + ah * 8192 + (i16 >> 2) * 2048 + (g & 1) * 1024 + ((g >> 1) * 8 + p8) * 64 + (i16 & 3) * 16;
        unsigned sl[4], sh[4];
        v4u ones;
#pragma unroll
        for (int q = 0; q < 4; ++q) {
            sl[q] = hq == q ? ((i16 & 1) ? 0x01000c0cu : 0x0c0c0100u) : 0x0c0c0c0cu;
            sh[q] = hq == q ? ((i16 & 1) ? 0x03020c0cu : 0x0c0c0302u) : 0x0c0c0c0cu;
            ones[q] = hq == q ? (hm & 0x3F803F80u) : 0u;
            PIN(sl[q]); PIN(sh[q]);
        }
        PIN(ones);
        PIN(xo); PIN(xe); PIN(qo); PIN(qpo);
        SB();
        const void* wqp = a.wq;
        OPQ(wsp); OPQ(xp); OPQ(wqp);
        const rsrc_t ws = mk(wsp, WS_BYTES);
        const rsrc_t rx = mk(xp, 2 * 576 * 2);
        const rsrc_t rq = mk(wqp, 576 * 4096 * 2);
        want = load_epoch(wsp);
        const v4u x0 = __builtin_amdgcn_raw_buffer_load_b128(rx, xo, 0, 0), x1 = __builtin_amdgcn_raw_buffer_load_b128(rx, xo + 16, 0, 0);
        const unsigned x2 = __builtin_amdgcn_raw_buffer_load_b32(rx, xo + 32, 0, 0);
        const v4u y0 = __builtin_amdgcn_raw_buffer_load_b128(rx, xe, 0, 0), y1 = __builtin_amdgcn_raw_buffer_load_b128(rx, xe + 16, 0, 0);
        const unsigned y2 = __builtin_amdgcn_raw_buffer_load_b32(rx, xe + 32, 0, 0);
        v4u wq[18];
#pragma unroll
        for (int j = 0; j < 18; ++j) wq[j] = __builtin_amdgcn_raw_buffer_load_b128(rq, qo, j * 8192, AUX_NT);
        SB();
        const unsigned xw[9] = {x0.x, x0.y, x0.z, x0.w, x1.x, x1.y, x1.z, x1.w, x2};
        const unsigned yw[9] = {y0.x, y0.y, y0.z, y0.w, y1.x, y1.y, y1.z, y1.w, y2};
        float s = 0.f, s2 = 0.f;
#pragma unroll
        for (int q = 0; q < 9; ++q) { s = dot2(xw[q], 0x3F803F80u, s); s2 = dot2(xw[q], xw[q], s2); }
        float mean = add16(rowsum(s)) * (1.0f / 576.0f);
        float rs = __builtin_amdgcn_rsqf(fmaxf(add16(rowsum(s2)) * (1.0f / 576.0f) - mean * mean, 0.f) + 1e-5f) * (0.08838834764831845f * 1.4426950408889634f);
        PIN(mean); PIN(rs);
        SB();
        f32x4 acc = Z4, cs = Z4;
#pragma unroll
        for (int j = 0; j < 18; ++j) {
            v4u af;
#pragma unroll
            for (int q = 0; q < 4; ++q) af[q] = __builtin_amdgcn_perm(0u, yw[j >> 1], (j & 1) ? sh[q] : sl[q]);
            cs = mfma32(ones, wq[j], cs);
            acc = mfma32(af, wq[j], acc);
        }
        const f32x4 v = (acc - cs * mean) * rs;
        st_c(ws, qpo, v4u{pk2(v[0], v[1]), want.x, pk2(v[2], v[3]), want.x});
        __builtin_amdgcn_s_setprio(0);
    } else {
        if (w == 4 || w == 5) {
            const int wb = w - 4, h = 8 * (xg >> 1) + ((2 * ai + wb + 2) & 7), crow = 64 * (4 * (xg & 1) + (ai >> 2));
            int qa = QA_OFF + (((g >> 1) * 32 + h) * 512 + crow + 4 * i16 + 2 * (g & 1)) * 2;
            PIN(qa);
            SB();
            const void* wukp = a.wuk;
            OPQ(wsp); OPQ(wukp);
            const rsrc_t ws = mk(wsp, WS_BYTES);
            st_c1(ws, qa, SENT);
            want = load_epoch(wsp);
            __builtin_amdgcn_s_sleep(B_DELAY);
            dma_loop(mks(wukp, 512 * 4096 * 2), 16, g * 8192, i16, 0xFEDCBA9876543210ull, crow * 8192 + h * 256, 4 * 8192,
                     L_UK + (w - 4) * 16384);
            VMW(16);
            if (lane == 0) st_c2(ws, FLQ_OFF + ((xg * 16 + ai) * 2 + wb) * 8, want);
            int po = QP_OFF + h * 8192 + i16 * 64 + g * 16;
            PIN(qa); PIN(po);
            VMW(1);
            v4u qf[8];
#pragma unroll
            for (int j = 0; j < 8; ++j) qf[j] = ld_c(ws, po + j * 1024);
            v4u uk[16];
#pragma unroll
            for (int tau = 0; tau < 4; ++tau)
#pragma unroll
                for (int s = 0; s < 4; ++s) uk[tau * 4 + s] = *(LAS v4u*)(L + L_UK + (w - 4) * 16384 + i16 * 1024 + tau * 256 + (((4 * s + g) ^ i16) * 16));
            for (unsigned s = 0; s < SPIN_CAP; ++s) {
                unsigned dd = 0;
#pragma unroll
                for (int j = 0; j < 8; ++j) dd |= (qf[j].y ^ want.x) | (qf[j].w ^ want.x);
                if (ALL(dd == 0u)) break;
                CBAR();
#pragma unroll
                for (int j = 0; j < 8; ++j) qf[j] = ld_c(ws, po + j * 1024);
            }
            f32x4 d[4] = {Z4, Z4, Z4, Z4};
#pragma unroll
            for (int s = 0; s < 4; ++s) {
                const v4u q = {qf[2 * s].x, qf[2 * s].z, qf[2 * s + 1].x, qf[2 * s + 1].z};
#pragma unroll
                for (int tau = 0; tau < 4; ++tau) d[tau] = mfma32(q, uk[tau * 4 + s], d[tau]);
            }
            const float e0 = rs16(sum4(d[0]), sum4(d[2])), e1 = rs16(sum4(d[1]), sum4(d[3]));
            st_c1(ws, qa, san(pk2(e0, e1)));
        } else {
            const int w3 = w < 4 ? w - 2 : (w < 6 ? 3 : w - 4);
            const int pf_ = FO_OFF + fu * 4608 + (w3 * 128 + lane) * 16;
            const int pe = AO_OFF + (2 * kg + rg) * 1152 + et * 128 + c8 * 16;
            const int lim = isF ? 288 - w3 * 128 : ((isE && w3 == 0) ? 16 : 0);
            int p0 = lane < lim ? (isF ? pf_ : pe) : OOB, p1 = lane + 64 < lim ? pf_ + 1024 : OOB;
            PIN(p0); PIN(p1);
            SB();
            OPQ(wsp);
            const rsrc_t ws = mk(wsp, WS_BYTES);
            st_c(ws, p0, SENT4);
            st_c(ws, p1, SENT4);
            want = load_epoch(wsp);
            VMW(0);
        }
    }
    OPQ(want);
    const rsrc_t ws = mk(wsp, WS_BYTES);
    const rsrc_t rx = mk(xp, 2 * 576 * 2);
    BAR();
    if (w == 7 && lane == 0) st_c2(ws, myfl, want);
    __builtin_amdgcn_s_sleep(1);

    if (isF) {
        unsigned xrow[3];
        {
            unsigned xl[3], xh[3];
#pragma unroll
            for (int i = 0; i < 3; ++i) {
                const int k = (tid & 255) + 256 * i;
                const int bb = k >= 288, kk = k - 288 * bb, m0 = bb * 576 + (kk >> 4) * 32 + (kk & 15);
                xl[i] = __builtin_amdgcn_raw_buffer_load_b16(rx, k < 576 ? m0 * 2 : OOB, 0, 0);
                xh[i] = __builtin_amdgcn_raw_buffer_load_b16(rx, k < 576 ? (m0 + 16) * 2 : OOB, 0, 0);
            }
            if ((fu & 7) != 3) for (int z_ = 0; z_ < F_DELAY; ++z_) __builtin_amdgcn_s_sleep(8);
#pragma unroll
            for (int i = 0; i < 3; ++i) { xrow[i] = xl[i] | (xh[i] << 16); PIN(xrow[i]); }
        }
        {
            const int key = ((rg >> 1) & 1) | ((w & 1) << 1);
            dma_loop(mks(a.wd, 4608 * 576 * 2), 9, rg * 1152 - (i16 & 8) * 16, i16 ^ (key << 1), 0ull, (64 * fu + 8 * w) * 1152, 128, L_DN + w * 1024, 8192);
        }
        const int keyg = (((w & 3) >> 1) & 1) | (((rg >> 2) & 1) << 1);
        const v4u sgu = mks((w >> 2) ? a.wu : a.wg, 576 * 4608 * 2);
        const int sgo = 8 * (w & 3) * 9216 + fu * 128;
        dma_loop(sgu, 10, rg * 9216 - (i16 & 8) * 16, i16 ^ (keyg << 1), 0ull, sgo, 32 * 9216, L_GU1 + w * 1024, 8192);
        VMW(10);
        BAR();
        v4u df[10];
        {
            const int key = ((tq >> 1) & 1) | ((g & 1) << 1);
            LAS unsigned char* tb = L + L_DN + (w >> 2) * 8192 + (8 * g + tq) * 128 + tp * 8 + (((w & 3) ^ key) * 32);
#pragma unroll
            for (int i = 0; i < 5; ++i)
#pragma unroll
                for (int s = 0; s < 2; ++s) {
                    LAS unsigned char* p = tb + (i < 4 ? i : (w < 4 ? 4 : 0)) * 16384 + s * 4096;
                    df[i * 2 + s] = trd(p, p + 512);
                }
        }
        BAR();
        const v4u fl2 = ld_c(ws, FL_OFF + 1024 + lane * 16);
        SB();
        dma_loop(sgu, 8, rg * 9216 - (i16 & 8) * 16, i16 ^ (keyg << 1), 0ull, sgo + 10 * 32 * 9216, 32 * 9216, L_GU2 + w * 1024, 8192);
        VMW(8);
        BAR();
        v4u gf[18], uf[18];
        const int keyp = (tq & 1) | ((g & 1) << 1);
        const int tro = (16 * (tq & 1) + 4 * g + (tq >> 1)) * 128 + tp * 8 + (((w & 3) ^ keyp) * 32);
        if (w < 4) {
#pragma unroll
            for (int s = 0; s < 10; ++s) {
                LAS unsigned char* p = L + L_GU1 + s * 8192 + tro;
                gf[s] = trd(p, p + 256);
                uf[s] = trd(p + 4096, p + 4096 + 256);
            }
        }
        BAR();
        f32x4 cg = Z4, cu = Z4;
        if (w >= 4) {
            const int t = tid - 256;
#pragma unroll
            for (int i = 0; i < 3; ++i) {
                const int k = t + 256 * i;
                if (k < 576) *(LAS unsigned*)(L + L_AO + 8 * AOP + k * 4 + (k >= 288 ? AOP - 1152 : 0)) = xrow[i];
            }
#pragma unroll
            for (int i = 0; i < 2; ++i) {
                const int k = t + 256 * i;
                if (k < 6 * 73) *(LAS v4u*)(L + L_AO + 10 * AOP + k * 16) = v4u{0u, 0u, 0u, 0u};
            }
        } else {
#pragma unroll
            for (int s = 0; s < 10; ++s) { cg = mfma32(ONES, gf[s], cg); cu = mfma32(ONES, uf[s], cu); }
            PIN(cg); PIN(cu);
        }
        VMW(0);
        wait_flags16(ws, FL_OFF + 1024 + lane * 16, fl2, want);
        BAR();
        float csg = 0.f, csu = 0.f;
        if (w < 4) {
#pragma unroll
            for (int s = 10; s < 18; ++s) {
                LAS unsigned char* p = L + L_GU2 + (s - 10) * 8192 + tro;
                gf[s] = trd(p, p + 256);
                uf[s] = trd(p + 4096, p + 4096 + 256);
            }
#pragma unroll
            for (int s = 10; s < 18; ++s) { cg = mfma32(ONES, gf[s], cg); cu = mfma32(ONES, uf[s], cu); }
            csg = cg[0]; csu = cu[0];
            PIN(csg); PIN(csu);
        } else {
            const int wq_ = w - 4;
            int go[3], lo_[3];
#pragma unroll
            for (int i = 0; i < 3; ++i) {
                const int id = i * 64 + lane;
                go[i] = id < 144 ? AO_OFF + wq_ * 2304 + id * 16 : OOB;
                lo_[i] = id < 144 ? L_AO + wq_ * 2 * AOP + id * 16 + (id >= 72 ? 16 : 0) : L_JUNK + lane * 16;
                PIN(lo_[i]);
            }
            v4u av[3];
            for (unsigned s = 0; s < SPIN_CAP; ++s) {
                CBAR();
#pragma unroll
                for (int i = 0; i < 3; ++i) av[i] = ld_c(ws, go[i]);
                if (ALL(umax(umax(mx4(av[0]), mx4(av[1])), mx4(av[2])) != SENT)) break;
            }
#pragma unroll
            for (int i = 0; i < 3; ++i) *(LAS v4u*)(L + lo_[i]) = av[i];
        }
        int fo = i16 < 2 ? FO_OFF + fu * 4608 + w * 128 + i16 * 64 + g * 16 : OOB;
        PIN(fo);
        BAR();
        LAS unsigned char* ab = L + L_AO + i16 * AOP + g * 16;
        f32x4 dg = Z4, du = Z4;
        if (w < 4) {
            v4u af[6];
#pragma unroll
            for (int s = 0; s < 6; ++s) af[s] = *(LAS v4u*)(ab + s * 64);
            SB();
#pragma unroll
            for (int s = 0; s < FBAR; ++s) {
                const v4u cur = af[s % 6];
                dg = mfma32(cur, gf[s], dg);
                du = mfma32(cur, uf[s], du);
                if (s + 6 < 18) af[s % 6] = *(LAS v4u*)(ab + (s + 6) * 64);
                SB();
            }
            BAR();
            const f32x4 s1 = *(LAS f32x4*)(L + L_FS + (g >> 1) * 32), s2 = *(LAS f32x4*)(L + L_FS + (g >> 1) * 32 + 16);
#pragma unroll
            for (int s = FBAR; s < (FBAR + 19) / 2; ++s) { dg = mfma32(af[s % 6], gf[s], dg); du = mfma32(af[s % 6], uf[s], du); }
            SB();
            const float mean = sum4(s1) * (1.0f / 576.0f);
            const float var = fmaxf(sum4(s2) * (1.0f / 576.0f) - mean * mean, 0.f);
            float rstd = __builtin_amdgcn_rsqf(var + 1e-5f);
            float og = -mean * csg * rstd, ou = -mean * csu * rstd;
#pragma unroll
            for (int s = (FBAR + 19) / 2; s < 18; ++s) { dg = mfma32(af[s % 6], gf[s], dg); du = mfma32(af[s % 6], uf[s], du); }
            PIN(rstd); PIN(og); PIN(ou);
            SB();
            const float gs = add16(rs32(dg[0] + dg[2], dg[1] + dg[3])), us = add16(rs32(du[0] + du[2], du[1] + du[3]));
            const float gg = gs * rstd + og, uu = us * rstd + ou;
            const float act = gg * uu * __builtin_amdgcn_rcpf(1.0f + __builtin_amdgcn_exp2f(gg * -1.4426950408889634f));
            if ((g & 1) == 0) *(LAS unsigned short*)(L + L_ACT + (g >> 1) * 128 + (16 * w + i16) * 2) = (unsigned short)pk2(act, act);
        } else {
            __builtin_amdgcn_s_setprio(2);
            const int wq_ = w - 4;
            v4u af[5];
#pragma unroll
            for (int i = 0; i < 5; ++i) af[i] = *(LAS v4u*)(ab + (i < 4 || wq_ < 2 ? wq_ + 4 * i : 0) * 64);
#pragma unroll
            for (int i = 0; i < 5; ++i) {
                if (i < 4 || wq_ < 2) {
                    dg = mfma32(af[i], af[i], dg);
                    du = mfma32(af[i], ONES, du);
                }
            }
            float s2 = (i16 & 1) ? dg[1] + dg[3] : dg[0] + dg[2];
            const float s1 = (i16 & 1) ? du[1] + du[3] : du[0] + du[2];
            s2 += dpp<DPP_ROR8>(s2); s2 += dpp<DPP_ROR4>(s2); s2 += dpp<DPP_ROR2>(s2);
            const float st = add32(rs16(s1, s2));
            if (lane < 32 && i16 < 2) *(LAS float*)(L + L_FS + ((i16 * 2 + g) * 4 + wq_) * 4) = st;
            __builtin_amdgcn_s_setprio(0);
            BAR();
        }
        BAR();
        {
            const v4u b0 = *(LAS v4u*)(L + L_ACT + (i16 & 1) * 128 + g * 16), b1 = *(LAS v4u*)(L + L_ACT + (i16 & 1) * 128 + 64 + g * 16);
#pragma unroll
            for (int i = 0; i < 5; ++i) {
                if (i < 4 || w < 4) {
                    f32x4 d = mfma32(df[2 * i + 1], b1, mfma32(df[2 * i], b0, Z4));
                    st_c(ws, fo + i * 1024, __builtin_bit_cast(v4u, d));
                }
            }
        }
        return;
    }
    if (!isE) {
        if (e >= 5 || xg == 3) {
            const int fs = e >= 5 ? 9 * xg + ((3 - xg) & 7) : 35;
            const int s0 = e >= 5 ? H_S0 + ((18 - H_S0) / 2) * (e - 5) : 9, ns = e >= 5 ? (18 - H_S0) / 2 : 9;
            const v4u sh_ = mks((w >> 2) ? a.wu : a.wg, 576 * 4608 * 2);
            int so_ = __builtin_amdgcn_readfirstlane((32 * s0 + 8 * (w & 3)) * 9216 + fs * 128);
            asm volatile("s_nop 4" : "+s"(so_));
            for (int i = 0; i < ns; ++i) {
                dmap(sh_, rg * 9216 + c8 * 16, so_, L_JUNK);
                so_ += 32 * 9216;
            }
        }
        if (blockIdx.x == 255 && w == 3) {
            check_flags(ws, want, lane);
            if (lane == 0) st_c2(ws, EPOCH_OFF, want);
        }
        return;
    }
    {
        const int hh = 8 * kg + w;
        for (int z_ = 0; z_ < E_DELAY; ++z_) __builtin_amdgcn_s_sleep(8);
        dma_loop(mks(a.wo, 4096 * 576 * 2), 16, rg * 1152 - (i16 & 8) * 16, i16 ^ (((rg >> 2) & 1) << 2), 0x2200220022002200ull,
                 hh * 128 * 1152 + et * 128, 8 * 1152, L_WO + w * 16384);
        if (w == 6) check_flags(ws, want, lane);
        VMW(0);
        v4u wf[16];
        {
            const int keyp = (tq & 1) | ((g & 1) << 1);
            LAS unsigned char* tb = L + L_WO + w * 16384 + (16 * (tq & 1) + 4 * g + (tq >> 1)) * 128 + tp * 8;
#pragma unroll
            for (int tau = 0; tau < 4; ++tau)
#pragma unroll
                for (int s = 0; s < 4; ++s) {
                    LAS unsigned char* p = tb + ((tau ^ keyp) * 32) + s * 4096;
                    wf[tau * 4 + s] = trd(p, p + 256);
                }
        }
        int ao = AO_OFF + (2 * kg + (lane >> 5)) * 1152 + et * 128 + (lane & 31) * 4;
        PIN(ao);
        BAR();
        v4u cf[4];
        const int co = i16 < 8 ? CX_OFF + hh * 2048 + i16 * 64 + g * 16 : OOB;
        for (unsigned s = 0; s < SPIN_CAP; ++s) {
            CBAR();
#pragma unroll
            for (int j = 0; j < 4; ++j) cf[j] = ld_c(ws, co + j * 512);
            if (ALL(umax(umax(mx4(cf[0]), mx4(cf[1])), umax(mx4(cf[2]), mx4(cf[3]))) != SENT)) break;
        }
        {
            f32x4 d[4] = {Z4, Z4, Z4, Z4};
#pragma unroll
            for (int s = 0; s < 4; ++s)
#pragma unroll
                for (int tau = 0; tau < 4; ++tau) d[tau] = mfma32(cf[s], wf[tau * 4 + s], d[tau]);
            const f32x4 ev = {rs16(d[0][0] + d[0][2], d[2][0] + d[2][2]), rs16(d[1][0] + d[1][2], d[3][0] + d[3][2]),
                              rs16(d[0][1] + d[0][3], d[2][1] + d[2][3]), rs16(d[1][1] + d[1][3], d[3][1] + d[3][3])};
            if (lane < 32) *(LAS f32x4*)(L + L_RE + w * 512 + lane * 16) = ev;
        }
        BAR();
        const int ru = 9 * kg + et;
        if (w == 0) {
            LAS unsigned char* p = L + L_RE + (lane >> 5) * 2048 + (lane & 31) * 16;
            const f32x4 v = (*(LAS f32x4*)p + *(LAS f32x4*)(p + 512)) + (*(LAS f32x4*)(p + 1024) + *(LAS f32x4*)(p + 1536));
            st_c1(ws, ao, san(pk2(rs32(v[0], v[2]), rs32(v[1], v[3]))));
        } else if (w == 1) {
            const v2u xr = __builtin_amdgcn_raw_buffer_load_b64(rx, rg == 4 ? ((c8 >> 2) * 576 + 16 * ru + 4 * (c8 & 3)) * 2 : OOB, 0, 0);
            __builtin_amdgcn_s_sleep(R_DELAY);
            v4u hv;
            const int ho = rg < 4 ? AO_OFF + (2 * rg + (c8 >> 2)) * 1152 + (ru >> 1) * 64 + (c8 & 3) * 16 : OOB;
            for (unsigned s = 0; s < SPIN_CAP; ++s) {
                CBAR();
                hv = ld_c(ws, ho);
                if (ALL(mx4(hv) != SENT)) break;
            }
            f32x4 acc = {bflo(xr.x), bfhi(xr.x), bflo(xr.y), bfhi(xr.y)};
#pragma unroll
            for (int j = 0; j < 4; ++j) acc[j] += (ru & 1) ? bfhi(hv[j]) : bflo(hv[j]);
            PIN(acc);
            v4u fv[9];
            const int fo = FO_OFF + rg * 4608 + ru * 128 + c8 * 16;
            for (unsigned s = 0; s < SPIN_CAP; ++s) {
                CBAR();
#pragma unroll
                for (int j = 0; j < 9; ++j) fv[j] = ld_c(ws, fo + j * 8 * 4608);
                unsigned m = mx4(fv[0]);
#pragma unroll
                for (int j = 1; j < 9; ++j) m = umax(m, mx4(fv[j]));
                if (ALL(m != SENT)) break;
            }
#pragma unroll
            for (int j = 0; j < 9; ++j) acc += __builtin_bit_cast(f32x4, fv[j]);
            float u0 = rs32(acc[0], acc[2]), u1 = rs32(acc[1], acc[3]);
            u0 = add16(u0); u1 = add16(u1);
            u0 += dpp<DPP_ROR8>(u0); u1 += dpp<DPP_ROR8>(u1);
            __builtin_amdgcn_raw_buffer_store_b32(pk2(u0, u1), mk(a.out, 2304), (lane & 31) < 8 ? ((c8 >> 2) * 576 + 16 * ru + 4 * (c8 & 3) + 2 * (lane >> 5)) * 2 : OOB, 0, 0);
        }
    }
}

}

extern "C" void kernel_launch(void* const* d_in, const int* in_sizes, int n_in, void* d_out, int out_size, void* d_ws,
                              size_t ws_size, hipStream_t stream) {
    Args a;
    a.x = d_in[0]; a.ckv = d_in[1]; a.wq = d_in[2]; a.wuk = d_in[3]; a.wuv = d_in[4]; a.wo = d_in[5];
    a.wg = d_in[7]; a.wu = d_in[8]; a.wd = d_in[9];
    a.out = d_out; a.ws = (unsigned char*)d_ws;
    (void)hipFuncSetAttribute((const void*)fused, hipFuncAttributeMaxDynamicSharedMemorySize, LDS_BYTES);
    hipLaunchKernelGGL(fused, dim3(256), dim3(NT), LDS_BYTES, stream, a);
}
```
